```python
import torch

def _softmax(x, axis=-1):
    return torch.softmax(x, dim=axis)
def _gelu(x):
    return torch.nn.functional.gelu(x, approximate="tanh")
def _silu(x):
    return torch.nn.functional.silu(x)
def _sigmoid(x):
    return torch.sigmoid(x)
def _layernorm(x, axis=-1, eps=1e-5):
    m = x.mean(dim=axis, keepdim=True)
    v = x.var(dim=axis, keepdim=True, unbiased=False)
    return (x - m) / torch.sqrt(v + eps)
def _scatter_add(tgt, idx, src, axis=0):
    return tgt.clone().index_add_(axis, idx, src)
def _segment_sum(src, seg, n):
    out = torch.zeros((n,) + src.shape[1:], dtype=src.dtype)
    return out.index_add_(0, seg, src)
def _pad(x, pw):
    flat = tuple(v for pair in reversed(pw) for v in pair)
    return torch.nn.functional.pad(x, flat)

kernel_name = 'parallel_sum__linear_attn_block__swiglu_ffn__prompt'

def setup_inputs():

    _w = (lambda a: (lambda t: t.to(torch.bfloat16) if t.is_floating_point() else t)(torch.as_tensor(a)))
    def _A_setup():
        B, S, D = 1, 2048, 640
        return {
            "x": _w((lambda *s: torch.randn(*s, dtype=torch.bfloat16) * 0.05)(B, S, D)),
            "wqkv": _w((lambda *s: torch.randn(*s, dtype=torch.bfloat16) * 0.2)(D, 3 * D)),
            "wo": _w((lambda *s: torch.randn(*s, dtype=torch.bfloat16) * 0.2)(D, D)),
        }
    _A_in = _A_setup()

    def _B_setup():
        B, S, D, F = 1, 2048, 640, 1728
        return {
            "x": _w((lambda *s: torch.randn(*s, dtype=torch.bfloat16) * 0.05)(B, S, D)),
            "w_gate": _w((lambda *s: torch.randn(*s, dtype=torch.bfloat16) * 0.05)(D, F)),
            "w_up": _w((lambda *s: torch.randn(*s, dtype=torch.bfloat16) * 0.05)(D, F)),
            "w_down": _w((lambda *s: torch.randn(*s, dtype=torch.bfloat16) * 0.05)(F, D)),
        }
    _B_in = _B_setup()

    return {
        "A_x": _A_in["x"],
        "A_wqkv": _A_in["wqkv"],
        "A_wo": _A_in["wo"],
        "B_x": _B_in["x"],
        "B_w_gate": _B_in["w_gate"],
        "B_w_up": _B_in["w_up"],
        "B_w_down": _B_in["w_down"],
    }

def reference(A_x, A_wqkv, A_wo, B_x, B_w_gate, B_w_up, B_w_down):
    def _A_ref(x, wqkv, wo):
        B, S, D = x.shape
        H = 32; Dh = D // H
        qkv = (lambda x, *d: x.permute(*d))((x @ wqkv).reshape(B, S, 3, H, Dh), 2, 0, 3, 1, 4)
        q, k, v = qkv[0], qkv[1], qkv[2]
        phi = lambda t: torch.where(t > 0, t + 1, torch.exp(t)) + 1e-6
        pq, pk = phi(q), phi(k)
        kv = torch.einsum("bhsd,bhse->bhde", pk, v)
        z = torch.einsum("bhsd->bhd", pk)
        num = torch.einsum("bhqd,bhde->bhqe", pq, kv)
        den = torch.einsum("bhqd,bhd->bhq", pq, z)[..., None] + 1e-6
        o = num / den
        return (lambda x, *d: x.permute(*d))(o, 0, 2, 1, 3).reshape(B, S, D) @ wo

    def _B_ref(x, w_gate, w_up, w_down):
        g = _silu(x @ w_gate)
        u = x @ w_up
        return (g * u) @ w_down

    x = A_x
    xn = _layernorm(x, axis=-1)
    return x + _A_ref(xn, A_wqkv, A_wo) + _B_ref(xn, B_w_gate, B_w_up, B_w_down)
```

```cpp
#include <hip/hip_runtime.h>
#include <hip/hip_bf16.h>
typedef __hip_bfloat16 bf16;
constexpr int S=2048, D=640, F=1728, H=32, DH=20;
__device__ inline float b2f(bf16 v){return __bfloat162float(v);}
__device__ inline float rb(float v){return __bfloat162float(__float2bfloat16(v));}

__global__ void k_ln(const bf16* x, bf16* xn){
  int r=blockIdx.x; __shared__ float red[64];
  float s=0,ss=0;
  for(int i=threadIdx.x;i<D;i+=64){float v=b2f(x[r*D+i]); s+=v;}
  for(int o=32;o>0;o>>=1) s+=__shfl_xor(s,o);
  float m=s/D;
  for(int i=threadIdx.x;i<D;i+=64){float v=b2f(x[r*D+i])-m; ss+=v*v;}
  for(int o=32;o>0;o>>=1) ss+=__shfl_xor(ss,o);
  float rs=rsqrtf(ss/D+1e-5f);
  for(int i=threadIdx.x;i<D;i+=64){xn[r*D+i]=__float2bfloat16((b2f(x[r*D+i])-m)*rs);}
}
__global__ void k_gemm(const bf16* A,const bf16* B,float* C,int M,int N,int K){
  __shared__ float As[16][17], Bs[16][17];
  int tx=threadIdx.x, ty=threadIdx.y; int row=blockIdx.y*16+ty, col=blockIdx.x*16+tx;
  float acc=0;
  for(int k0=0;k0<K;k0+=16){
    As[ty][tx]=b2f(A[row*K+k0+tx]); Bs[ty][tx]=b2f(B[(k0+ty)*N+col]);
    __syncthreads();
    for(int k=0;k<16;k++) acc+=As[ty][k]*Bs[k][tx];
    __syncthreads();
  }
  C[row*N+col]=acc;
}
__device__ inline float phi(float t){return (t>0? t+1.f: __expf(t))+1e-6f;}
__global__ void k_kv(const float* qkv,float* kv,float* z){
  int h=blockIdx.x; int t=threadIdx.x;
  if(t<400){int d=t/20,e=t%20; float a=0; for(int s=0;s<S;s++) a+=rb(phi(qkv[s*1920+640+h*20+d]))*rb(qkv[s*1920+1280+h*20+e]); kv[h*400+t]=a;}
  else if(t<420){int d=t-400; float a=0; for(int s=0;s<S;s++) a+=rb(phi(qkv[s*1920+640+h*20+d])); z[h*20+d]=a;}
}
__global__ void k_o(const float* qkv,const float* kv,const float* z,bf16* o){
  int s=blockIdx.x; int c=threadIdx.x; int h=c/20,e=c%20; float num=0,den=0;
  for(int d=0;d<20;d++){float p=rb(phi(qkv[s*1920+h*20+d])); num+=p*kv[h*400+d*20+e]; den+=p*z[h*20+d];}
  o[s*D+c]=__float2bfloat16(num/(den+1e-6f));
}
__global__ void k_h(const float* g,const float* u,bf16* hh){
  int i=blockIdx.x*256+threadIdx.x; float a=g[i]; hh[i]=__float2bfloat16(a/(1.f+__expf(-a))*u[i]);
}
__global__ void k_out(const bf16* x,const float* a,const float* b,bf16* out){
  int i=blockIdx.x*256+threadIdx.x; out[i]=__float2bfloat16(b2f(x[i])+a[i]+b[i]);
}
extern "C" void kernel_launch(void* const* d_in, const int* in_sizes, int n_in,
                              void* d_out, int out_size, void* d_ws, size_t ws_size,
                              hipStream_t stream) {
  const bf16* x=(const bf16*)d_in[0]; const bf16* wqkv=(const bf16*)d_in[1]; const bf16* wo=(const bf16*)d_in[2];
  const bf16* wg=(const bf16*)d_in[4]; const bf16* wu=(const bf16*)d_in[5]; const bf16* wd=(const bf16*)d_in[6];
  char* ws=(char*)d_ws;
  bf16* xn=(bf16*)ws; ws+=S*D*2;
  float* qkv=(float*)ws; ws+=S*1920*4;
  float* g=(float*)ws; ws+=S*F*4;
  float* u=(float*)ws; ws+=S*F*4;
  float* kv=(float*)ws; ws+=H*400*4;
  float* z=(float*)ws; ws+=H*32*4;
  bf16* o=(bf16*)ws; ws+=S*D*2;
  bf16* hh=(bf16*)ws; ws+=S*F*2;
  float* a=(float*)ws; ws+=S*D*4;
  float* b=(float*)ws; ws+=S*D*4;
  k_ln<<<S,64,0,stream>>>(x,xn);
  k_gemm<<<dim3(1920/16,S/16),dim3(16,16),0,stream>>>(xn,wqkv,qkv,S,1920,D);
  k_gemm<<<dim3(F/16,S/16),dim3(16,16),0,stream>>>(xn,wg,g,S,F,D);
  k_gemm<<<dim3(F/16,S/16),dim3(16,16),0,stream>>>(xn,wu,u,S,F,D);
  k_kv<<<H,448,0,stream>>>(qkv,kv,z);
  k_o<<<S,640,0,stream>>>(qkv,kv,z,o);
  k_h<<<S*F/256,256,0,stream>>>(g,u,hh);
  k_gemm<<<dim3(D/16,S/16),dim3(16,16),0,stream>>>(o,wo,a,S,D,D);
  k_gemm<<<dim3(D/16,S/16),dim3(16,16),0,stream>>>(hh,wd,b,S,D,F);
  k_out<<<S*D/256,256,0,stream>>>(x,a,b,(bf16*)d_out);
}
```

```cpp
#include <hip/hip_runtime.h>
#include <hip/hip_bf16.h>

typedef unsigned u32;
typedef unsigned long long u64;
typedef short s16x4 __attribute__((ext_vector_type(4)));
typedef short bf16x8 __attribute__((ext_vector_type(8)));
typedef __bf16 bf2 __attribute__((ext_vector_type(2)));
typedef float f32x2 __attribute__((ext_vector_type(2)));
typedef float f32x4 __attribute__((ext_vector_type(4)));
typedef u32 u32x2 __attribute__((ext_vector_type(2)));
typedef u32 u32x4 __attribute__((ext_vector_type(4)));
#define LDS3 __attribute__((address_space(3)))
#define GAS __attribute__((address_space(1)))
#define DI __device__ __forceinline__
#define SB() __builtin_amdgcn_sched_barrier(0)
#define RLX_AGENT __ATOMIC_RELAXED, __HIP_MEMORY_SCOPE_AGENT

constexpr u32 WS_NONCE = 0, WS_F0 = 1024, WS_F1 = 2048, WS_F1KV = 3072, WS_F2 = 4096, WS_STATS = 8192;
constexpr u32 WS_KVP = 32768, KVP_STRIDE = 6784;
constexpr u32 WS_W2T = 524288, W2T_STRIDE = 380 * 1024;
constexpr u32 WS_HF = 4194304, WS_PQ = 14680064, WS_END = WS_PQ + 128 * 20 * 1024;
constexpr u32 WS_DBG = 20u << 20;
constexpr u32 L_MISC = 131072, L_STATS = 157952, L_F0 = 161024, L_CS = 161024, L_GO = 163584, L_TOTAL = 163840;
constexpr u32 KVT_ROW = 208;
constexpr int SL0 = 4;
constexpr u32 SPIN_CAP = 1u << 16;

struct Params {
  const char *x, *wqkv, *wo, *wg, *wu, *wd;
  char *out, *ws;
};

#define STAMP(k) do {} while (0)

template <int IMM> DI void glds(u32 voff, const char* sbase, u32 lds) {
  asm volatile("s_add_u32 m0, %2, %3\n\ts_nop 0\n\tglobal_load_lds_dwordx4 %0, %1 sc1" ::"v"(voff), "s"(sbase), "s"(lds), "i"(IMM) : "memory", "scc");
}
template <int N> DI void wait_vm() { asm volatile("s_waitcnt vmcnt(%0)" ::"i"(N) : "memory"); }
template <int N> DI void sync_vm() { asm volatile("s_waitcnt vmcnt(%0) lgkmcnt(0)\n\ts_barrier" ::"i"(N) : "memory"); }
DI void sync_lds() { asm volatile("s_waitcnt lgkmcnt(0)\n\ts_barrier" ::: "memory"); }
template <int AUX> DI void st16(u32x4 v, __amdgpu_buffer_rsrc_t rs, u32 voff, u32 soff) {
  __builtin_amdgcn_raw_buffer_store_b128(v, rs, voff, soff, AUX);
  asm volatile("s_nop 1" ::"v"(v) : "memory");
}
DI u32 opaque(u32 v) { asm volatile("" : "+v"(v)); return v; }
DI bf16x8 lds128(u32 a) { return *(const LDS3 bf16x8*)(size_t)a; }
DI f32x4 ldsf4(u32 a) { return *(const LDS3 f32x4*)(size_t)a; }
DI bf16x8 ldtr(u32 a0, u32 a1) {
  s16x4 lo = __builtin_amdgcn_ds_read_tr16_b64_v4i16((LDS3 s16x4*)(size_t)a0);
  s16x4 hi = __builtin_amdgcn_ds_read_tr16_b64_v4i16((LDS3 s16x4*)(size_t)a1);
  return __builtin_shufflevector(lo, hi, 0, 1, 2, 3, 4, 5, 6, 7);
}
DI f32x4 mfma(bf16x8 a, bf16x8 b, f32x4 c) { return __builtin_amdgcn_mfma_f32_16x16x32_bf16(a, b, c, 0, 0, 0); }
DI u32 pk2(float a, float b) { f32x2 f = {a, b}; return __builtin_bit_cast(u32, __builtin_convertvector(f, bf2)); }
DI float fragsum(bf16x8 f, float acc) {
  u32x4 v = __builtin_bit_cast(u32x4, f);
#pragma unroll
  for (int i = 0; i < 4; i++) {
    const u32 e = v[i];
    acc = __builtin_amdgcn_fdot2_f32_bf16(__builtin_bit_cast(bf2, e), __builtin_bit_cast(bf2, 0x3f803f80u), acc, false);
  }
  return acc;
}
template <int C> DI float dppf(float v) { return __builtin_bit_cast(float, __builtin_amdgcn_update_dpp(0, __builtin_bit_cast(int, v), C, 0xf, 0xf, true)); }
DI float sum_rows(float v) {
  u32 u = __builtin_bit_cast(u32, v);
  auto a = __builtin_amdgcn_permlane16_swap(u, u, false, false);
  v = __builtin_bit_cast(float, (u32)a[0]) + __builtin_bit_cast(float, (u32)a[1]);
  u = __builtin_bit_cast(u32, v);
  auto b = __builtin_amdgcn_permlane32_swap(u, u, false, false);
  return __builtin_bit_cast(float, (u32)b[0]) + __builtin_bit_cast(float, (u32)b[1]);
}
DI float sum_wave(float v) {
  v += dppf<0xB1>(v); v += dppf<0x4E>(v); v += dppf<0x141>(v); v += dppf<0x140>(v);
  return sum_rows(v);
}
DI float blo(u32 v) { return __builtin_bit_cast(float, v << 16); }
DI float bhi(u32 v) { return __builtin_bit_cast(float, v & 0xffff0000u); }
DI float phi(float t, float rs) {
  float e = __builtin_amdgcn_fmed3f(__builtin_amdgcn_exp2f(t * (rs * 1.44269504f)), 0.f, 1.f);
  return __builtin_fmaf(__builtin_fmaxf(t, 0.f), rs, e) + 1e-6f;
}
DI void poll(const char* addr, bool active, u32 nonce) {
#pragma unroll 1
  for (u32 spins = 0; spins < SPIN_CAP; ++spins) {
    u32 v = __hip_atomic_load((GAS u32*)addr, RLX_AGENT);
    if (__all(!active || v == nonce)) break;
    __builtin_amdgcn_s_sleep(1);
  }
  asm volatile("" ::: "memory");
}
DI u32 w2_pos(int ks) {
  int gw, s;
  if (ks < 54) { gw = ks < 14 ? 0 : ks < 28 ? 1 : ks < 41 ? 2 : 3; s = ks - (gw == 0 ? 0 : gw == 1 ? 14 : gw == 2 ? 27 : 40); }
  else { gw = (ks - 54) / 5; s = 14 + (ks - 54) % 5; }
  return (s >= SL0 && s < SL0 + 6) ? gw * 30 + (s - SL0) * 5 : 120 + gw * 65 + (s < SL0 ? s : s - 6) * 5;
}

struct Ph0 { u32x4 xa; u32 xb, nonce; };
DI void ph0_issue(const Params& p, int b, int w, int lane, Ph0& r) {
  const char* row = p.x + (size_t)(8 * b + w) * 1280;
  asm volatile("global_load_dword %0, %3, %4 sc1\n\tglobal_load_dwordx4 %1, %5, %6\n\tglobal_load_dword %2, %7, %6 offset:1024"
               : "=&v"(r.nonce), "=&v"(r.xa), "=&v"(r.xb)
               : "v"(0), "s"(p.ws), "v"(lane * 16), "s"(row), "v"(lane * 4) : "memory");
  const int uid = b + 256 * (w - 2);
  if (w >= 2 && uid < 740) {
    const int ks = uid / 10, line = uid % 10;
    const char* src = (ks < 54 ? p.wd + (size_t)ks * (32 * 1280) : p.wo + (size_t)(ks - 54) * (32 * 1280)) + line * 128;
    const u32 vo = (lane >> 3) * 1280 + (lane & 7) * 16, l = L_MISC + (w - 2) * 4096;
    glds<0>(vo, src, l); glds<1024>(vo, src + 8 * 1280, l); glds<2048>(vo, src + 16 * 1280, l); glds<3072>(vo, src + 24 * 1280, l);
  }
}
template <int NY> DI u32 ph0_finish(const Params& p, __amdgpu_buffer_rsrc_t rws, int b, int w, int lane, Ph0& r) {
  asm volatile("s_waitcnt vmcnt(%3)" : "+v"(r.nonce), "+v"(r.xa), "+v"(r.xb) : "i"(NY) : "memory");
  float v[10];
#pragma unroll
  for (int i = 0; i < 4; i++) { v[2 * i] = blo(r.xa[i]); v[2 * i + 1] = bhi(r.xa[i]); }
  v[8] = blo(r.xb); v[9] = bhi(r.xb);
  float s = 0.f;
#pragma unroll
  for (int i = 0; i < 10; i++) s += v[i];
  const float mean = sum_wave(s) * (1.f / 640.f);
  float q = 0.f;
#pragma unroll
  for (int i = 0; i < 10; i++) { float d = v[i] - mean; q = __builtin_fmaf(d, d, q); }
  const float rs = __builtin_amdgcn_rsqf(sum_wave(q) * (1.f / 640.f) + 1e-5f);
  if (lane == 0) {
    u64 o = (u64)__builtin_bit_cast(u32, mean) | ((u64)__builtin_bit_cast(u32, rs) << 32);
    __hip_atomic_store((GAS u64*)(p.ws + WS_STATS + (8 * b + w) * 8), o, RLX_AGENT);
  }
  const int uid = b + 256 * (w - 2);
  if (w >= 2 && uid < 740) {
    const int ks = uid / 10, line = uid % 10, q4 = lane >> 4, qq = (lane >> 2) & 3, pp = lane & 3;
    const bool nat = ks >= 54 && (ks - 54) % 5 == 2;
    const u32 a0 = L_MISC + (w - 2) * 4096 + ((nat ? 8 : 4) * q4 + qq) * 128 + pp * 8, a1 = a0 + (nat ? 4 : 16) * 128;
    const u32 pos = w2_pos(ks);
#pragma unroll
    for (int t = 0; t < 4; t++) {
      const int ct = 4 * line + t, j = ct / 5, tj = ct % 5;
      bf16x8 f = ldtr(a0 + t * 32, a1 + t * 32);
      st16<16>(__builtin_bit_cast(u32x4, f), rws, lane * 16, WS_W2T + j * W2T_STRIDE + (pos + tj) * 1024);
    }
  }
  return __builtin_amdgcn_readfirstlane(r.nonce) + 1u;
}

DI void hook_publish0(const Params& p, int b, int w, int lane, u32 nonce) {
  if (w == 0 && lane == 0) __hip_atomic_store((GAS u32*)(p.ws + WS_F0 + b * 4), nonce, RLX_AGENT);
}
DI void hook_fetch0(const Params& p, int w, int lane) {
  if (w == 0) glds<0>(lane * 16, p.ws + WS_F0, L_F0);
}
DI void hook_check0(const Params& p, int b, int w, int lane, u32 nonce, int row0) {
  u32x4 f = *(const LDS3 u32x4*)(size_t)(L_F0 + lane * 16);
  if (!__all(f[0] == nonce && f[1] == nonce && f[2] == nonce && f[3] == nonce)) {
#pragma unroll 1
    for (int k = 0; k < 4; k++) poll(p.ws + WS_F0 + k * 256 + lane * 4, true, nonce);
  }
  asm volatile("" ::: "memory");
  if (w < 3) glds<0>(lane * 16, p.ws + WS_STATS + row0 * 8 + w * 1024, L_STATS + w * 1024);
  if (b == 0 && w == 7 && lane == 0) __hip_atomic_store((GAS u32*)(p.ws + WS_NONCE), nonce, RLX_AGENT);
}

template <int NU>
DI u32 p1_ffn(const Params& p, __amdgpu_buffer_rsrc_t rws, int b, int w, int lane, int strip, int rt, const char* w2t) {
  constexpr u32 XA = 0, XAP = 24576, XB = 49152, XBP = 20480, WW = 90112, WWP = 16384;
  constexpr int NBX = NU == 6 ? 2 : 3, NV = 3 + NBX + 2;
  const int rg = w >> 1, cg = w & 1, r = lane & 15, q = lane >> 4;
  const int ustart = (rt >= 3 ? 64 : 0) + (rt % 3 == 0 ? 0 : rt % 3 == 1 ? 22 : 43);
  const bool full = rt % 3 == 0;
  const bool isq = strip >= 27;
  const u32 rstride = isq ? 3840 : 3456;
  const char* swa = isq ? p.wqkv + (strip - 27) * 256 : p.wg + strip * 128;
  const char* swb = isq ? swa + 128 : p.wu + strip * 128;
  const char* sxa = p.x + (size_t)ustart * (16 * 1280);
  const char* sxb = sxa;
  u32 dxa = 128, dxb = 128, dw = 64 * rstride;

  u32 va[3], vb[NBX], vw;
  const int pb0 = w < 2 ? 2 * w : w < 6 ? 3 * w - 2 : 2 * w + 4;
#pragma unroll
  for (int n = 0; n < 3; n++) {
    const int i = 3 * w + n, slot = i >> 1, g = slot / 3, unit = (g == 0 ? 0 : 5 * g + 1) + slot % 3, r16 = (i & 1) * 8 + (lane >> 3);
    va[n] = (unit * 16 + r16) * 1280 + (((lane & 7) ^ (r16 >> 1)) * 16);
  }
#pragma unroll
  for (int n = 0; n < NBX; n++) {
    const int i = pb0 + n, slot = i >> 1, r16 = (i & 1) * 8 + (lane >> 3);
    int unit = slot < 3 ? slot + 3 : slot < 5 ? slot + 6 : slot < 7 ? slot + 9 : slot + 12;
    if (!full && unit == 21) unit = 20;
    vb[n] = (unit * 16 + r16) * 1280 + (((lane & 7) ^ (r16 >> 1)) * 16);
  }
  {
    const int row = 8 * w + (lane >> 3), f = ((row >> 1) & 1) | (((row >> 3) & 1) << 1);
    vw = row * rstride + ((((lane & 7) >> 1) ^ f) * 32) + (lane & 1) * 16;
  }
  const u32 la = XA + 3 * w * 1024, lb = XB + pb0 * 1024, lw = WW + w * 1024;
  const u32 xs = r * 128 + ((q ^ (r >> 1)) * 16);
  const u32 xa0 = opaque(XA + rg * 6144 + xs), xa1 = opaque(xa0 ^ 64);
  const u32 xb0 = opaque(XB + (rg == 0 ? 0 : 2 * rg + 1) * 2048 + xs), xb1 = opaque(xb0 ^ 64);
  const int qq = (lane >> 2) & 3, pp = lane & 3, ff = (qq >> 1) | ((q & 1) << 1);
  const u32 wt0 = opaque(WW + (8 * q + qq) * 128 + (((2 * cg) ^ ff) * 32) + pp * 8), wt1 = opaque(wt0 ^ 32);

#define GA(P, n) glds<(P) * XAP + (n) * 1024>(va[n], sxa, la)
#define GBX(P, n) glds<(P) * XBP + (n) * 1024>(vb[n], sxb, lb)
#define GW(P, m) glds<(P) * WWP + (m) * 8192>(vw, (m) ? swb : swa, lw)
#define LDXA(X, P, u) do { X[0] = lds128(xa0 + (P) * XAP + (u) * 2048); X[1] = lds128(xa1 + (P) * XAP + (u) * 2048); } while (0)
#define LDXB(X, P, u) do { X[0] = lds128(xb0 + (P) * XBP + (u) * 2048); X[1] = lds128(xb1 + (P) * XBP + (u) * 2048); } while (0)
#define LDW(W, P, n) W[(n) >> 2][(n) & 3] = ldtr(((n) & 1 ? wt1 : wt0) + (P) * WWP + (((n) >> 1) & 1) * 8192 + ((n) >> 2) * 4096, \
                                               ((n) & 1 ? wt1 : wt0) + (P) * WWP + (((n) >> 1) & 1) * 8192 + ((n) >> 2) * 4096 + 512)
#define MM(u, ks, W, X) do { _Pragma("unroll") for (int t = 0; t < 4; t++) acc[u][t] = mfma(W[ks][t], X[ks], acc[u][t]); } while (0)

  Ph0 ph;
  ph0_issue(p, b, w, lane, ph);
  GW(0, 0); GW(0, 1); swa += dw; swb += dw;
  GA(0, 0); GA(0, 1); GA(0, 2); sxa += dxa;
  GBX(0, 0); GBX(0, 1); if constexpr (NBX == 3) GBX(0, 2); sxb += dxb;
  GW(1, 0); GW(1, 1); swa += dw; swb += dw;
  GA(1, 0); GA(1, 1); GA(1, 2); sxa += dxa;
  if (w == 0 && lane == 0) *(volatile LDS3 u32*)(size_t)L_GO = 0u;
  const u32 nonce = ph0_finish<NV + 5>(p, rws, b, w, lane, ph);
  STAMP(1);
  f32x4 acc[NU][4];
#pragma unroll
  for (int u = 0; u < NU; u++)
#pragma unroll
    for (int t = 0; t < 4; t++) acc[u][t] = f32x4{0.f, 0.f, 0.f, 0.f};
  float cs[4] = {0.f, 0.f, 0.f, 0.f};
  bf16x8 W0[2][4], W1[2][4], X0[2], X1[2];
  sync_vm<NV + 3>();
#pragma unroll
  for (int n = 0; n < 8; n++) LDW(W0, 0, n);
  sync_vm<NV>();
  LDXA(X0, 0, 0);
  GBX(1, 0);
  STAMP(2);
  const bool last_on = NU == 5 || rg == 0 || full;

#define CHUNK(P, Wc, Wn, Xa, Xb, HOOK_Y, HOOK_X)                                                              \
  do {                                                                                                       \
    LDXA(Xb, P, 1); SB(); MM(0, 0, Wc, Xa); SB(); GBX(1 - P, 1); SB(); MM(0, 1, Wc, Xa); SB();                \
    if constexpr (NBX == 3) GBX(1 - P, 2);                                                                    \
    sxb += dxb;                                                                                               \
    if (rg == 2 * (P)) { _Pragma("unroll") for (int t = 0; t < 4; t++) cs[t] = fragsum(Wc[0][t], cs[t]); }    \
    LDXA(Xa, P, 2); SB(); MM(1, 0, Wc, Xb); SB(); GW(P, 0); SB(); MM(1, 1, Wc, Xb); SB(); GW(P, 1);           \
    swa += dw; swb += dw;                                                                                     \
    if (rg == 2 * (P) + 1) { _Pragma("unroll") for (int t = 0; t < 4; t++) cs[t] = fragsum(Wc[1][t], cs[t]); }\
    SB(); sync_vm<NV>(); SB();                                                                                \
    HOOK_Y;                                                                                                   \
    if constexpr (NU == 6) {                                                                                  \
      LDXB(Xb, P, 0); LDW(Wn, 1 - P, 0); LDW(Wn, 1 - P, 1); LDW(Wn, 1 - P, 2); SB();                          \
      MM(2, 0, Wc, Xa); SB(); GA(P, 0); SB(); MM(2, 1, Wc, Xa); SB();                                         \
      LDXB(Xa, P, 1); LDW(Wn, 1 - P, 3); LDW(Wn, 1 - P, 4); LDW(Wn, 1 - P, 5); SB();                          \
      MM(3, 0, Wc, Xb); SB(); GA(P, 1); SB(); MM(3, 1, Wc, Xb); SB();                                         \
      LDXB(Xb, P, 2); LDW(Wn, 1 - P, 6); LDW(Wn, 1 - P, 7); SB();                                             \
      MM(4, 0, Wc, Xa); SB(); GA(P, 2); SB(); MM(4, 1, Wc, Xa); SB();                                         \
      sxa += dxa;                                                                                             \
      sync_vm<NV>(); SB();                                                                                    \
      HOOK_X;                                                                                                 \
      LDXA(Xa, 1 - P, 0); SB();                                                                               \
      if (last_on) { MM(NU - 1, 0, Wc, Xb); MM(NU - 1, 1, Wc, Xb); }                                          \
      SB(); GBX(P, 0); SB();                                                                                  \
    } else {                                                                                                  \
      LDXB(Xb, P, 0); LDW(Wn, 1 - P, 0); LDW(Wn, 1 - P, 1); LDW(Wn, 1 - P, 2); LDW(Wn, 1 - P, 3); SB();       \
      MM(2, 0, Wc, Xa); SB(); GA(P, 0); SB(); MM(2, 1, Wc, Xa); SB(); GA(P, 1); SB();                         \
      LDXB(Xa, P, 1); LDW(Wn, 1 - P, 4); LDW(Wn, 1 - P, 5); LDW(Wn, 1 - P, 6); LDW(Wn, 1 - P, 7); SB();       \
      MM(3, 0, Wc, Xb); SB(); GA(P, 2); SB(); MM(3, 1, Wc, Xb); SB();                                         \
      sxa += dxa;                                                                                             \
      sync_vm<NV>(); SB();                                                                                    \
      HOOK_X;                                                                                                 \
      LDXA(Xb, 1 - P, 0); SB();                                                                               \
      MM(NU - 1, 0, Wc, Xa); SB(); GBX(P, 0); SB(); MM(NU - 1, 1, Wc, Xa); SB();                              \
    }                                                                                                         \
  } while (0)

#pragma unroll 1
  for (int it = 0; it < 5; it++) {
    if (it == 4) {
      swa = w2t + WW; swb = swa + 8192; dw = WWP; vw = w * 1024 + lane * 16;
      sxa = w2t + XA; dxa = XAP;
#pragma unroll
      for (int n = 0; n < 3; n++) va[n] = (3 * w + n) * 1024 + lane * 16;
    }
#define HY0 do { if (it == 4) { sxb = w2t + XB; dxb = XBP; _Pragma("unroll") for (int n = 0; n < NBX; n++) vb[n] = (pb0 + n) * 1024 + lane * 16; } } while (0)
#define HY1 do { if (it == 3) hook_check0(p, b, w, lane, nonce, ustart * 16); } while (0)
#define HX1 do { if (it == 0) hook_publish0(p, b, w, lane, nonce); if (it == 2) hook_fetch0(p, w, lane); } while (0)
    CHUNK(0, W0, W1, X0, X1, HY0, (void)0);
    if constexpr (NU == 6) CHUNK(1, W1, W0, X0, X1, HY1, HX1);
    else CHUNK(1, W1, W0, X1, X0, HY1, HX1);
  }
  GBX(1, 1);
  if constexpr (NBX == 3) GBX(1, 2);
  STAMP(3);
#undef CHUNK
#undef GA
#undef GBX
#undef GW
#undef LDXA
#undef LDXB
#undef LDW
#undef MM
#undef HY0
#undef HY1
#undef HX1

#pragma unroll
  for (int t = 0; t < 4; t++) {
    const float v = sum_rows(cs[t]);
    if (q == 0) *(LDS3 float*)(size_t)(L_CS + (rg * 128 + cg * 64 + t * 16 + r) * 4) = v;
  }
  sync_lds();
  f32x4 c4[4];
#pragma unroll
  for (int t = 0; t < 4; t++) {
    const u32 a = L_CS + (cg * 64 + t * 16 + 4 * q) * 4;
    c4[t] = (ldsf4(a) + ldsf4(a + 512)) + (ldsf4(a + 1024) + ldsf4(a + 1536));
  }
  const int ub = rg == 0 ? 0 : 5 * rg + 1;
#pragma unroll
  for (int u = 0; u < NU; u++) {
    if (u == 5 && !last_on) break;
    const f32x2 st = *(const LDS3 f32x2*)(size_t)(L_STATS + ((ub + u) * 16 + r) * 8);
    const float nm = -st[0], rs = st[1];
    const int ug = ustart + ub + u;
    if (!isq) {
      const float c1 = rs * -1.44269504f, cv = __builtin_amdgcn_rcpf(rs * rs);
      float h[8];
#pragma unroll
      for (int t = 0; t < 2; t++)
#pragma unroll
        for (int i = 0; i < 4; i++) {
          const float g = __builtin_fmaf(nm, c4[t][i], acc[u][t][i]), up = __builtin_fmaf(nm, c4[t + 2][i], acc[u][t + 2][i]);
          const float e = __builtin_amdgcn_exp2f(g * c1);
          h[4 * t + i] = g * up * __builtin_amdgcn_rcpf(__builtin_fmaf(e, cv, cv));
        }
      u32x4 o = {pk2(h[0], h[1]), pk2(h[2], h[3]), pk2(h[4], h[5]), pk2(h[6], h[7])};
      st16<16>(o, rws, lane * 16, WS_HF + (ug * 74 + 2 * strip + cg) * 1024);
    } else {
#pragma unroll
      for (int m = 0; m < 2; m++) {
        float h[8];
#pragma unroll
        for (int t = 0; t < 2; t++)
#pragma unroll
          for (int i = 0; i < 4; i++) h[4 * t + i] = phi(__builtin_fmaf(nm, c4[2 * m + t][i], acc[u][2 * m + t][i]), rs);
        u32x4 o = {pk2(h[0], h[1]), pk2(h[2], h[3]), pk2(h[4], h[5]), pk2(h[6], h[7])};
        st16<16>(o, rws, lane * 16, WS_PQ + (ug * 20 + 4 * (strip - 27) + 2 * m + cg) * 1024);
      }
    }
  }
  return nonce;
}

DI u32 p1_kv(const Params& p, __amdgpu_buffer_rsrc_t rws, int b, int w, int lane, int g, int rt8, const char* w2t) {
  constexpr u32 XA = 0, XP = 16384, XB = 32768, WW = 65536, WWP = 32768;
  constexpr int NV = 8;
  const int rg = w >> 1, cg = w & 1, r = lane & 15, q = lane >> 4;
  const char* sw = p.wqkv + 1280 + ((160 * g) & ~127);
  const char* sxa = p.x + (size_t)rt8 * (256 * 1280);
  const char* sxb = sxa + 32 * 1280;
  u32 dxa = 128, dxb = 128, dw = 64 * 3840;
  u32 va[2], vb[2], vw[4];
#pragma unroll
  for (int n = 0; n < 2; n++) {
    const int r16 = n * 8 + (lane >> 3);
    va[n] = vb[n] = ((4 * rg + cg) * 16 + r16) * 1280 + (((lane & 7) ^ (r16 >> 1)) * 16);
  }
  {
    const int row = 8 * w + (lane >> 3), f = ((row >> 1) & 1) | (((row >> 3) & 1) << 1);
    const u32 v = row * 3840 + ((((lane & 7) >> 1) ^ f) * 32) + (lane & 1) * 16;
    vw[0] = v; vw[1] = v + 128; vw[2] = v + 1280; vw[3] = v + 1408;
  }
  const u32 la = XA + 2 * w * 1024, lb = XB + 2 * w * 1024, lw = WW + w * 1024;
  const u32 xa0 = opaque(XA + rg * 4096 + r * 128 + ((q ^ (r >> 1)) * 16)), xa1 = opaque(xa0 ^ 64);
  const int qq = (lane >> 2) & 3, pp = lane & 3, ff = (qq >> 1) | ((q & 1) << 1);
  u32 wt[3];
#pragma unroll
  for (int n = 0; n < 3; n++) {
    const int cb = ((160 * g) & 127) + 80 * cg + (n == 0 ? 8 * pp : n == 1 ? 40 + 8 * pp : 32 + 40 * (pp & 1));
    wt[n] = opaque(WW + ((n == 2 ? (pp >> 1) * 2 : 0) + (cb >> 7)) * 8192 + (8 * q + qq) * 128 + ((((cb >> 5) & 3) ^ ff) * 32) + (cb & 31));
  }
#define GA(P, n) glds<(P) * XP + (n) * 1024>(va[n], sxa, la)
#define GBX(P, n) glds<(P) * XP + (n) * 1024>(vb[n], sxb, lb)
#define GW(P, m) glds<(P) * WWP + (m) * 8192>(vw[m], sw, lw)
#define LDX(X, P, u) do { X[0] = lds128(xa0 + ((u) >> 1) * XB + (P) * XP + ((u) & 1) * 2048); X[1] = lds128(xa1 + ((u) >> 1) * XB + (P) * XP + ((u) & 1) * 2048); } while (0)
#define WADDR(P, ks, t) (wt[(t) == 4 ? 2 : (t) & 1] + (P) * WWP + ((t) == 2 || (t) == 3 ? 16384 : 0) + (ks) * 4096)
#define LDW(W, P, ks) do { _Pragma("unroll") for (int t = 0; t < 5; t++) W[ks][t] = ldtr(WADDR(P, ks, t), WADDR(P, ks, t) + 512); } while (0)
#define MM(u, ks, W, X) do { _Pragma("unroll") for (int t = 0; t < 5; t++) acc[u][t] = mfma(X[ks], W[ks][t], acc[u][t]); } while (0)

  Ph0 ph;
  ph0_issue(p, b, w, lane, ph);
  GW(0, 0); GW(0, 1); GW(0, 2); GW(0, 3); sw += dw;
  GA(0, 0); GA(0, 1); sxa += dxa;
  GBX(0, 0); GBX(0, 1); sxb += dxb;
  GW(1, 0); GW(1, 1); GW(1, 2); GW(1, 3); sw += dw;
  GA(1, 0); GA(1, 1); sxa += dxa;
  if (w == 0 && lane == 0) *(volatile LDS3 u32*)(size_t)L_GO = 0u;
  const u32 nonce = ph0_finish<NV + 6>(p, rws, b, w, lane, ph);
  STAMP(1);
  f32x4 acc[4][5];
#pragma unroll
  for (int u = 0; u < 4; u++)
#pragma unroll
    for (int t = 0; t < 5; t++) acc[u][t] = f32x4{0.f, 0.f, 0.f, 0.f};
  float cs[5] = {0.f, 0.f, 0.f, 0.f, 0.f};
  bf16x8 W0[2][5], W1[2][5], X0[2], X1[2];
  sync_vm<NV + 2>();
  LDW(W0, 0, 0); LDW(W0, 0, 1);
  sync_vm<NV>();
  LDX(X0, 0, 0);
  GBX(1, 0); GBX(1, 1); sxb += dxb;
  STAMP(2);

#define CHUNK(P, Wc, Wn, HOOK_Y, HOOK_X)                                                                      \
  do {                                                                                                       \
    LDX(X1, P, 1); SB(); GW(P, 0); SB(); MM(0, 0, Wc, X0); SB(); GW(P, 1); GW(P, 2); SB(); MM(0, 1, Wc, X0); SB(); GW(P, 3); \
    sw += dw;                                                                                                 \
    if (rg == 2 * (P)) { _Pragma("unroll") for (int t = 0; t < 5; t++) cs[t] = fragsum(Wc[0][t], cs[t]); }    \
    SB(); sync_vm<NV>(); SB();                                                                                \
    HOOK_Y;                                                                                                   \
    LDX(X0, P, 2); LDW(Wn, 1 - P, 0); SB(); MM(1, 0, Wc, X1); SB(); GA(P, 0); SB(); MM(1, 1, Wc, X1); SB();   \
    if (rg == 2 * (P) + 1) { _Pragma("unroll") for (int t = 0; t < 5; t++) cs[t] = fragsum(Wc[1][t], cs[t]); }\
    LDX(X1, P, 3); LDW(Wn, 1 - P, 1); SB(); MM(2, 0, Wc, X0); SB(); GA(P, 1); SB(); MM(2, 1, Wc, X0); SB();   \
    sxa += dxa;                                                                                               \
    sync_vm<NV>(); SB();                                                                                      \
    HOOK_X;                                                                                                   \
    LDX(X0, 1 - P, 0); SB(); MM(3, 0, Wc, X1); SB(); GBX(P, 0); SB(); MM(3, 1, Wc, X1); SB(); GBX(P, 1); SB();\
    sxb += dxb;                                                                                               \
  } while (0)

#pragma unroll 1
  for (int it = 0; it < 5; it++) {
    if (it == 4) {
      sw = w2t + WW; dw = WWP; sxa = w2t + XA; dxa = XP; sxb = w2t + XB; dxb = XP;
#pragma unroll
      for (int m = 0; m < 4; m++) vw[m] = m * 8192 + w * 1024 + lane * 16;
#pragma unroll
      for (int n = 0; n < 2; n++) va[n] = vb[n] = (2 * w + n) * 1024 + lane * 16;
    }
#define HY1 do { if (it == 3) hook_check0(p, b, w, lane, nonce, rt8 * 256); } while (0)
#define HX1 do { if (it == 0) hook_publish0(p, b, w, lane, nonce); if (it == 2) hook_fetch0(p, w, lane); } while (0)
    CHUNK(0, W0, W1, (void)0, (void)0);
    CHUNK(1, W1, W0, HY1, HX1);
  }
  STAMP(3);
#undef CHUNK
#undef GA
#undef GBX
#undef GW
#undef LDX
#undef WADDR
#undef LDW
#undef MM
#undef HY1
#undef HX1

#pragma unroll
  for (int t = 0; t < 5; t++) {
    const float v = sum_rows(cs[t]);
    if (q == 0) *(LDS3 float*)(size_t)(L_CS + (rg * 160 + cg * 80 + t * 16 + r) * 4) = v;
  }
  sync_lds();
#pragma unroll
  for (int t = 0; t < 5; t++) {
    const LDS3 float* a = (const LDS3 float*)(size_t)(L_CS + (cg * 80 + t * 16 + r) * 4);
    cs[t] = (a[0] + a[160]) + (a[320] + a[480]);
  }
  const bool isk = r < 8;
  f32x4 R[7];
#pragma unroll
  for (int n = 0; n < 7; n++) R[n] = f32x4{0.f, 0.f, 0.f, 0.f};
#pragma unroll
  for (int kk = 0; kk < 2; kk++) {
    u32x4 T[5];
#pragma unroll
    for (int uu = 0; uu < 2; uu++) {
      const int u = 2 * kk + uu;
      const u32 a = L_STATS + ((4 * rg + u) * 16 + 4 * q) * 8;
      const f32x4 s0 = ldsf4(a), s1 = ldsf4(a + 16);
      const float mean[4] = {s0[0], s0[2], s1[0], s1[2]}, rs[4] = {s0[1], s0[3], s1[1], s1[3]};
#pragma unroll
      for (int t = 0; t < 5; t++) {
        float y[4];
#pragma unroll
        for (int i = 0; i < 4; i++) {
          const float tt = __builtin_fmaf(-mean[i], cs[t], acc[u][t][i]);
          y[i] = t < 2 ? phi(tt, rs[i]) : t < 4 ? tt * rs[i] : (isk ? phi(tt, rs[i]) : tt * rs[i]);
        }
        T[t][2 * uu] = pk2(y[0], y[1]); T[t][2 * uu + 1] = pk2(y[2], y[3]);
      }
    }
    u32x4 T4b;
#pragma unroll
    for (int i = 0; i < 4; i++) T4b[i] = isk ? 0x3f803f80u : T[4][i];
#define BC(v) __builtin_bit_cast(bf16x8, v)
    R[0] = mfma(BC(T[0]), BC(T[2]), R[0]); R[1] = mfma(BC(T[1]), BC(T[3]), R[1]);
    R[2] = mfma(BC(T[0]), BC(T4b), R[2]);  R[3] = mfma(BC(T[1]), BC(T4b), R[3]);
    R[4] = mfma(BC(T[4]), BC(T[2]), R[4]); R[5] = mfma(BC(T[4]), BC(T[3]), R[5]);
    R[6] = mfma(BC(T[4]), BC(T4b), R[6]);
  }
  {
    const u32 base = L_MISC + rg * 6720 + cg * 3360;
    auto put = [&](int head, int e, int d, f32x4 v) { *(LDS3 f32x4*)(size_t)(base + head * 1680 + e * 80 + d * 4) = v; };
    const int ea = r == 0 ? 20 : r + 8, eb = r == 0 ? 20 : r + 4;
    const bool va_ = r == 0 || (r >= 8 && r < 12), vb_ = r == 0 || r >= 12;
    put(0, r, 4 * q, R[0]); put(1, r, 4 * q, R[1]);
    if (va_) put(0, ea, 4 * q, R[2]);
    if (vb_) put(1, eb, 4 * q, R[3]);
    if (q == 0) put(0, r, 16, R[4]);
    if (q == 1) put(1, r, 16, R[5]);
    if ((q == 0 && va_) || (q == 1 && vb_)) put(q, q ? eb : ea, 16, R[6]);
  }
  sync_lds();
  const int tid = w * 64 + lane;
  if (tid < 420) {
    const u32 a = L_MISC + tid * 16;
    const f32x4 v = (ldsf4(a) + ldsf4(a + 6720)) + (ldsf4(a + 13440) + ldsf4(a + 20160));
    st16<16>(__builtin_bit_cast(u32x4, v), rws, tid * 16, WS_KVP + (g * 8 + rt8) * KVP_STRIDE);
  }
  return nonce;
}

__global__ __launch_bounds__(512, 2) void fused_block(Params p) {
  __shared__ __attribute__((aligned(16))) char smem[L_TOTAL];
  asm volatile("" ::"v"((LDS3 char*)smem) : "memory");
  const int b = blockIdx.x, lane = threadIdx.x & 63, w = __builtin_amdgcn_readfirstlane(threadIdx.x >> 6);
  const int xcd = b & 7, idx = b >> 3;
  const int ti = 8 * (xcd >> 1) + (idx >> 2), tj = 4 * (xcd & 1) + (idx & 3);
  const __amdgpu_buffer_rsrc_t rws = __builtin_amdgcn_make_buffer_rsrc(p.ws, 0, WS_END, 0x00020000);
  const char* w2t = p.ws + WS_W2T + tj * W2T_STRIDE;
  STAMP(0);
  u32 nonce, flag_off;
  if (idx < 24) {
    const int strip = (xcd & 3) + 4 * (idx / 3), rt = 3 * (xcd >> 2) + idx % 3;
    if (w < 2 || w >= 6) nonce = p1_ffn<6>(p, rws, b, w, lane, strip, rt, w2t);
    else nonce = p1_ffn<5>(p, rws, b, w, lane, strip, rt, w2t);
    flag_off = WS_F1 + rt * 128 + strip * 4;
  } else {
    const int g = 2 * (xcd & 3) + ((idx - 24) >> 2), rt8 = 4 * (xcd >> 2) + (idx & 3);
    nonce = p1_kv(p, rws, b, w, lane, g, rt8, w2t);
    flag_off = WS_F1KV + g * 128 + rt8 * 4;
  }
  STAMP(4);
  sync_vm<0>();
  STAMP(5);
  const __amdgpu_buffer_rsrc_t rx = __builtin_amdgcn_make_buffer_rsrc((void*)p.x, 0, 2048 * 1280, 0x00020000);
  const u32 xso = ti * (64 * 1280) + tj * 160;
  u32x4 xr0, xr1;
  u32 id0r, id0c, id1r, id1c;
#define ITEMS(tid) do { id0r = (tid) / 10; id0c = (tid) % 10; id1r = ((tid) + 512) / 10; id1c = ((tid) + 512) % 10; } while (0)
  if (w >= 4) {
    const int r = lane & 15, q = lane >> 4, tid = w * 64 + lane;
    if (w == 4 && lane == 0) __hip_atomic_store((GAS u32*)(p.ws + flag_off), nonce, RLX_AGENT);
    const int th = tid - 256, hw = w - 4, s0 = (80 * tj) >> 5;
#pragma unroll 1
    for (int k = th; k < 84 * 24; k += 256) {
      const int row = k / 24, sl = k % 24, hh = row < 80 ? row / 20 : row - 80;
      const int loc = 8 * (s0 + (sl >> 3)) + (sl & 1) * 4 + ((sl >> 1) & 3) - 20 * tj - 5 * hh;
      if (loc < 0 || loc >= 5) *(LDS3 u32x2*)(size_t)(L_MISC + row * KVT_ROW + sl * 8) = u32x2{0u, 0u};
    }
    poll(p.ws + WS_F1KV + tj * 128 + (lane & 7) * 4, true, nonce);
    STAMP(6);
#pragma unroll
    for (int n = 0; n < 2; n++) {
      const int id = th + 256 * n;
      if (id < 420) {
        u32x4 v[8];
#pragma unroll
        for (int k = 0; k < 8; k++) v[k] = __builtin_amdgcn_raw_buffer_load_b128(rws, id * 16, WS_KVP + (tj * 8 + k) * KVP_STRIDE, 16);
        f32x4 s = __builtin_bit_cast(f32x4, v[0]);
#pragma unroll
        for (int k = 1; k < 8; k++) s += __builtin_bit_cast(f32x4, v[k]);
        const int hh = id / 105, rem = id % 105, e = rem / 5, c4 = 20 * tj + 5 * hh + rem % 5, row = e < 20 ? hh * 20 + e : 80 + hh;
        *(LDS3 u32x2*)(size_t)(L_MISC + row * KVT_ROW + ((c4 >> 3) - s0) * 64 + (c4 & 3) * 16 + ((c4 >> 2) & 1) * 8) = u32x2{pk2(s[0], s[1]), pk2(s[2], s[3])};
      }
    }
    STAMP(7);
    if (w == 4) {
      const int uu = 4 * ti + (lane >> 5) * 3, ul = uu & 63, rt = (uu >> 6) * 3 + (ul < 22 ? 0 : ul < 43 ? 1 : 2);
      poll(p.ws + WS_F1 + rt * 128 + (lane & 31) * 4, true, nonce);
    }
    sync_lds();
    STAMP(8);
    const int ug = 4 * ti + hw;
    bf16x8 pq[3];
#pragma unroll
    for (int s = 0; s < 3; s++) pq[s] = __builtin_bit_cast(bf16x8, __builtin_amdgcn_raw_buffer_load_b128(rws, lane * 16, WS_PQ + (ug * 20 + s0 + s) * 1024, 16));
    f32x4 den = {0.f, 0.f, 0.f, 0.f};
#pragma unroll
    for (int s = 0; s < 3; s++) den = mfma(lds128(L_MISC + (80 + (r & 3)) * KVT_ROW + s * 64 + q * 16), pq[s], den);
    float inv[4];
#pragma unroll
    for (int i = 0; i < 4; i++) inv[i] = __builtin_amdgcn_rcpf(den[i] + 1e-6f);
    u32 od[5][2];
#pragma unroll
    for (int t = 0; t < 5; t++) {
      const int nt = (tj & 1) ? (t + 1) % 5 : t;
      f32x4 num = {0.f, 0.f, 0.f, 0.f};
#pragma unroll
      for (int s = 0; s < 3; s++) num = mfma(lds128(L_MISC + (16 * nt + r) * KVT_ROW + s * 64 + q * 16), pq[s], num);
      const int ch = 4 * nt + q;
      const float iv = ch < 5 ? inv[0] : ch < 10 ? inv[1] : ch < 15 ? inv[2] : inv[3];
      od[t][0] = pk2(num[0] * iv, num[1] * iv); od[t][1] = pk2(num[2] * iv, num[3] * iv);
    }
    const u32 ob = WS_HF + (ug * 74 + 54 + 5 * (tj >> 1)) * 1024, op = ob + (tj & 1) * 3072;
    st16<16>(u32x4{od[0][0], od[0][1], od[1][0], od[1][1]}, rws, lane * 16, op);
    st16<16>(u32x4{od[2][0], od[2][1], od[3][0], od[3][1]}, rws, lane * 16, op + 1024);
    auto e0 = __builtin_amdgcn_permlane16_swap(od[4][0], od[4][0], false, false);
    auto e1 = __builtin_amdgcn_permlane16_swap(od[4][1], od[4][1], false, false);
    if (!(q & 1)) st16<16>(u32x4{e0[0], e1[0], e0[1], e1[1]}, rws, (q >> 1) * 256 + r * 16, ob + 2048 + (tj & 1) * 512);
    wait_vm<0>();
    if (lane == 0) __hip_atomic_store((GAS u32*)(p.ws + WS_F2 + ti * 128 + (hw * 8 + tj) * 4), nonce, RLX_AGENT);
    STAMP(9);
    if (w == 4) {
      poll(p.ws + WS_F2 + ti * 128 + (lane & 31) * 4, true, nonce);
      if (lane == 0) *(volatile LDS3 u32*)(size_t)L_GO = 1u;
    }
    STAMP(10);
    ITEMS(tid);
    xr0 = __builtin_amdgcn_raw_buffer_load_b128(rx, id0r * 1280 + id0c * 16, xso, 0);
    xr1 = xr0;
  } else {
    const u32 bo = WS_W2T + tj * W2T_STRIDE + (120 + w * 65) * 1024;
    const u32 ao_f = WS_HF + (4 * ti * 74 + (w == 0 ? 0 : 13 * w + 1)) * 1024, ao_a = WS_HF + (4 * ti * 74 + 54 + 5 * w) * 1024;
    const u32 v16 = opaque(lane * 16), bl = opaque(w * 30720 + v16);
    bf16x8 A[19][4], B[19][5];
    f32x4 acc[4][5];
#pragma unroll
    for (int u = 0; u < 4; u++)
#pragma unroll
      for (int t = 0; t < 5; t++) acc[u][t] = f32x4{0.f, 0.f, 0.f, 0.f};
#define LB(s) do { _Pragma("unroll") for (int t = 0; t < 5; t++) B[s][t] = __builtin_bit_cast(bf16x8, __builtin_amdgcn_raw_buffer_load_b128(rws, v16, bo + (((s) < SL0 ? (s) : (s) - 6) * 5 + t) * 1024, 16)); } while (0)
#define LL(s) do { _Pragma("unroll") for (int t = 0; t < 5; t++) B[s][t] = lds128(bl + (((s) - SL0) * 5 + t) * 1024); } while (0)
#define LA(s) do { _Pragma("unroll") for (int u = 0; u < 4; u++) A[s][u] = __builtin_bit_cast(bf16x8, __builtin_amdgcn_raw_buffer_load_b128(rws, v16, ((s) < 14 ? ao_f + (s) * 1024 : ao_a + ((s) - 14) * 1024) + u * 74 * 1024, 16)); } while (0)
#define ST(s) do { SB(); _Pragma("unroll") for (int u = 0; u < 4; u++) _Pragma("unroll") for (int t = 0; t < 5; t++) acc[u][t] = mfma(B[s][t], A[s][u], acc[u][t]); SB(); } while (0)
    const bool has0 = w < 2;
#pragma unroll
    for (int t = 0; t < 5; t++) B[0][t] = bf16x8{0, 0, 0, 0, 0, 0, 0, 0};
#pragma unroll
    for (int u = 0; u < 4; u++) A[0][u] = bf16x8{0, 0, 0, 0, 0, 0, 0, 0};
    if (has0) LB(0);
    LB(1); LB(2); LB(3);
    SB();
    asm volatile("s_barrier" ::: "memory");
    STAMP(8);
    SB();
    if (has0) LA(0);
    LA(1); LA(2); LA(3);
    if (has0) ST(0);
    SB();
    LA(4); LA(5);          ST(1);
    LA(6); LA(7);          ST(2);
    LA(8); LL(4);          ST(3);
    LL(5); LA(9);  LB(10); ST(4);
    LL(6); LA(10);         ST(5);
    LL(7); LB(11);         ST(6);
    LL(8);                 ST(7);
    LL(9); LA(11);         ST(8);
    LB(12); LA(12);        ST(9);
    LB(13); LA(13);        ST(10);
    LB(14);
    STAMP(9);
    while (*(volatile LDS3 u32*)(size_t)L_GO == 0u) __builtin_amdgcn_s_sleep(1);
    asm volatile("" ::: "memory");
    STAMP(10);
    LA(14);                ST(11);
    LB(15); LA(15);        ST(12);
    LB(16); LA(16);        ST(13);
    LB(17); LA(17);        ST(14);
    LB(18); LA(18);        ST(15);
    const u32 tid = w * 64 + (opaque(v16) >> 4);
    ITEMS(tid);
    xr0 = __builtin_amdgcn_raw_buffer_load_b128(rx, id0r * 1280 + id0c * 16, xso, 0);
    xr1 = __builtin_amdgcn_raw_buffer_load_b128(rx, id1r * 1280 + id1c * 16, xso, 0);
    ST(16); ST(17); ST(18);
    const u32 r = tid & 15, q = (tid >> 4) & 3;
#undef LB
#undef LL
#undef LA
#undef ST
    STAMP(11);
#pragma unroll
    for (int u = 0; u < 4; u++)
#pragma unroll
      for (int t = 0; t < 5; t++) *(LDS3 f32x4*)(size_t)(w * 30720 + (16 * u + r) * 336 + (16 * t + 4 * q) * 4) = acc[u][t];
  }
  sync_lds();
  STAMP(12);
  const __amdgpu_buffer_rsrc_t ro = __builtin_amdgcn_make_buffer_rsrc(p.out, 0, 2048 * 1280, 0x00020000);
#pragma unroll
  for (int n = 0; n < 2; n++) {
    if (n == 1 && w >= 2) break;
    const u32 ir = n ? id1r : id0r, ic = n ? id1c : id0c, a = ir * 336 + ic * 32;
    const f32x4 lo = (ldsf4(a) + ldsf4(a + 30720)) + (ldsf4(a + 61440) + ldsf4(a + 92160));
    const f32x4 hi = (ldsf4(a + 16) + ldsf4(a + 30736)) + (ldsf4(a + 61456) + ldsf4(a + 92176));
    const u32x4 xr = n ? xr1 : xr0;
    u32x4 o = {pk2(lo[0] + blo(xr[0]), lo[1] + bhi(xr[0])), pk2(lo[2] + blo(xr[1]), lo[3] + bhi(xr[1])),
               pk2(hi[0] + blo(xr[2]), hi[1] + bhi(xr[2])), pk2(hi[2] + blo(xr[3]), hi[3] + bhi(xr[3]))};
    st16<0>(o, ro, ir * 1280 + ic * 16, xso);
  }
  STAMP(13);
}

extern "C" void kernel_launch(void* const* d_in, const int* in_sizes, int n_in, void* d_out, int out_size, void* d_ws,
                              size_t ws_size, hipStream_t stream) {
  Params p;
  p.x = (const char*)d_in[0]; p.wqkv = (const char*)d_in[1]; p.wo = (const char*)d_in[2];
  p.wg = (const char*)d_in[4]; p.wu = (const char*)d_in[5]; p.wd = (const char*)d_in[6];
  p.out = (char*)d_out; p.ws = (char*)d_ws;
  fused_block<<<256, 512, 0, stream>>>(p);
}
```

```cpp
#include <hip/hip_runtime.h>
#include <hip/hip_bf16.h>

typedef unsigned u32;
typedef unsigned long long u64;
typedef short s16x4 __attribute__((ext_vector_type(4)));
typedef short bf16x8 __attribute__((ext_vector_type(8)));
typedef __bf16 bf2 __attribute__((ext_vector_type(2)));
typedef float f32x2 __attribute__((ext_vector_type(2)));
typedef float f32x4 __attribute__((ext_vector_type(4)));
typedef u32 u32x2 __attribute__((ext_vector_type(2)));
typedef u32 u32x4 __attribute__((ext_vector_type(4)));
#define LDS3 __attribute__((address_space(3)))
#define GAS __attribute__((address_space(1)))
#define DI __device__ __forceinline__
#define SB() __builtin_amdgcn_sched_barrier(0)
#define RLX_AGENT __ATOMIC_RELAXED, __HIP_MEMORY_SCOPE_AGENT

constexpr u32 FS = 256;
constexpr u32 WS_NONCE = 0, WS_STATS = 8192, WS_F0 = 32768, WS_F1KV = 49152, WS_F1 = 65536, WS_F2 = 131072;
constexpr u32 WS_KVP = 393216, KVP_STRIDE = 4096;
constexpr u32 WS_W2T = 655360, W2T_STRIDE = 370 * 1024;
constexpr u32 WS_HF = WS_W2T + 8 * W2T_STRIDE, WS_PQ = WS_HF + 128 * 74 * 1024, WS_END = WS_PQ + 128 * 20 * 1024;
constexpr u32 WS_DBG = 20u << 20;
constexpr u32 L_MISC = 131072, L_STATS = 157952, L_CS = 161024, L_GO = 163584, L_CNT = 163588, L_GO0 = 163592, L_FREE = 163600, L_NONCE = 163616, L_JUNK = 163328, L_ROWB = 159744, L_TOTAL = 163840;
constexpr u32 L_KVT = L_STATS, L_RED = 122880;
#define STY "XRRLLLLLLLRSRRLLLLL"
#define PUTPRIO 0
constexpr char STYPE[20] = STY;
constexpr int srank(int s) {
  int n = STYPE[s] == 'S' ? 5 : 0;
  for (int i = s >= 14 ? 14 : 0; i < s; i++) n += STYPE[i] == STYPE[s];
  return n;
}
#define SWAP 0
#define EXN 2
constexpr int NEX = 4 * EXN;
constexpr int NK = 15 - NEX, KE = 1;
#define QALL 0
#define FD 0
#define KMASK 1
#define PSLEEP 2
#define OAUX 16
#define WSOFF 0
#define W2AUX ""
#define RT0 0
#define TW1 0
#define TW2 3
#define TPOS 0
#define SROW (xcd * 256 + (b & ~7) + w)
#define UID (opaque_s0(b) * 4 + (w - 2))
#define QRAW 0
#define NP1 10
#define WIN 10
constexpr u32 SPIN_CAP = 1u << 16;

#define WAUX ""
#define XAUX ""
#define RAUX ""
struct Params {
  const char *x, *wqkv, *wo, *wg, *wu, *wd;
  char *out, *ws;
};

#define STP
#define STA
#define STAMP(k) do {} while (0)
#define STAMP_FLUSH() do {} while (0)
#define LSTAMP(k) do {} while (0)

template <int IMM, int K = 0> DI void glds(u32 voff, const char* sbase, u32 lds) {
#define GL(A) asm volatile("s_add_u32 m0, %2, %3\n\ts_nop 0\n\tglobal_load_lds_dwordx4 %0, %1 " A ::"v"(voff), "s"(sbase), "s"(lds), "i"(IMM) : "memory", "scc")
  if constexpr (K == 0) GL("sc1"); else if constexpr (K == 1) GL(WAUX); else if constexpr (K == 2) GL(XAUX); else if constexpr (K == 3) GL(RAUX); else GL(W2AUX);
#undef GL
}
DI void glds4(u32 voff, const char* sbase, u32 lds) {
  asm volatile("s_mov_b32 m0, %2\n\ts_nop 0\n\tglobal_load_lds_dword %0, %1 sc1" ::"v"(voff), "s"(sbase), "s"(lds) : "memory");
}
DI void st_flag(const char* addr, u32 val) {
  asm volatile("s_nop 4\n\ts_mov_b64 exec, 1\n\tglobal_store_dword %0, %1, %2 sc1\n\ts_mov_b64 exec, -1" ::"v"(0), "v"(val), "s"(addr) : "memory");
}
template <int N> DI void wait_vm() { asm volatile("s_waitcnt vmcnt(%0)" ::"i"(N) : "memory"); }
template <int N> DI void sync_vm() {
  __builtin_amdgcn_s_waitcnt(0xC07F);
  asm volatile("s_waitcnt vmcnt(%0) lgkmcnt(0)\n\ts_barrier" ::"i"(N) : "memory"); }
DI void sync_lds() { asm volatile("s_waitcnt lgkmcnt(0)\n\ts_barrier" ::: "memory"); }
template <int AUX> DI void st16(u32x4 v, __amdgpu_buffer_rsrc_t rs, u32 voff, u32 soff) {
  __builtin_amdgcn_raw_buffer_store_b128(v, rs, voff, soff, AUX);
  asm volatile("s_nop 1" ::"v"(v) : "memory");
}
DI u32 opaque(u32 v) { asm volatile("" : "+v"(v)); return v; }
DI const char* opaque_p(const char* v) { asm volatile("" : "+s"(v)); return v; }
DI void pin(f32x4& v) { asm volatile("" : "+v"(v)); }
DI u32 opaque_s0(u32 v) { asm volatile("" : "+s"(v)); return v; }
DI u32 opaque_s(u32 v) { v = __builtin_amdgcn_readfirstlane(v); asm volatile("" : "+s"(v)); return v; }
DI bf16x8 lds128(u32 a) {
  return *(const LDS3 bf16x8*)(size_t)a; }
DI f32x4 ldsf4(u32 a) { return *(const LDS3 f32x4*)(size_t)a; }
DI bf16x8 ldtr(u32 a0, u32 a1) {
  s16x4 lo = __builtin_amdgcn_ds_read_tr16_b64_v4i16((LDS3 s16x4*)(size_t)a0);
  s16x4 hi = __builtin_amdgcn_ds_read_tr16_b64_v4i16((LDS3 s16x4*)(size_t)a1);
  return __builtin_shufflevector(lo, hi, 0, 1, 2, 3, 4, 5, 6, 7);
}
DI f32x4 mfma(bf16x8 a, bf16x8 b, f32x4 c) {
  return __builtin_amdgcn_mfma_f32_16x16x32_bf16(a, b, c, 0, 0, 0); }
DI void mfma_ip(f32x4& c, bf16x8 a, bf16x8 b) {
  asm volatile("v_mfma_f32_16x16x32_bf16 %0, %1, %2, %0" : "+v"(c) : "v"(a), "v"(b));
}
#define SETPRIO(n) __builtin_amdgcn_s_setprio(n)
DI u32 pk2(float a, float b) { f32x2 f = {a, b}; return __builtin_bit_cast(u32, __builtin_convertvector(f, bf2)); }
template <int C> DI float dppf(float v) { return __builtin_bit_cast(float, __builtin_amdgcn_update_dpp(0, __builtin_bit_cast(int, v), C, 0xf, 0xf, true)); }
DI float sum_rows(float v) {
  u32 u = __builtin_bit_cast(u32, v);
  auto a = __builtin_amdgcn_permlane16_swap(u, u, false, false);
  v = __builtin_bit_cast(float, (u32)a[0]) + __builtin_bit_cast(float, (u32)a[1]);
  u = __builtin_bit_cast(u32, v);
  auto b = __builtin_amdgcn_permlane32_swap(u, u, false, false);
  return __builtin_bit_cast(float, (u32)b[0]) + __builtin_bit_cast(float, (u32)b[1]);
}
DI float sum_wave(float v) {
  v += dppf<0xB1>(v); v += dppf<0x4E>(v); v += dppf<0x141>(v); v += dppf<0x140>(v);
  return sum_rows(v);
}
DI float blo(u32 v) { return __builtin_bit_cast(float, v << 16); }
DI float bhi(u32 v) { return __builtin_bit_cast(float, v & 0xffff0000u); }
DI f32x2 splat(float v) { return f32x2{v, v}; }
DI f32x2 half(f32x4 v, int h) { return h ? __builtin_shufflevector(v, v, 2, 3) : __builtin_shufflevector(v, v, 0, 1); }
DI f32x2 fma2(f32x2 a, f32x2 b, f32x2 c) { return __builtin_elementwise_fma(a, b, c); }
DI f32x2 exp2c(f32x2 a) { return f32x2{__builtin_amdgcn_fmed3f(__builtin_amdgcn_exp2f(a[0]), 0.f, 1.f), __builtin_amdgcn_fmed3f(__builtin_amdgcn_exp2f(a[1]), 0.f, 1.f)}; }
DI f32x2 phi2(f32x2 a, float lo, f32x2 e) { return fma2(f32x2{__builtin_fmaxf(a[0], lo), __builtin_fmaxf(a[1], lo)}, splat(0.693147181f), e); }
DI float mul1(float a, float b) { float r; asm("v_mul_f32 %0, %1, %2" : "=v"(r) : "v"(a), "v"(b)); return r; }
DI float add1(float a, float b) { float r; asm volatile("v_add_f32 %0, %1, %2" : "=v"(r) : "v"(a), "v"(b)); return r; }
DI float phi1(float t) {
  float e, m;
  asm("v_mul_f32 %0, 0x3fb8aa3b, %2\n\tv_exp_f32 %0, %0 clamp\n\tv_max_f32 %1, 0x358637bd, %2\n\tv_add_f32 %0, %0, %1" : "=&v"(e), "=&v"(m) : "v"(t));
  return e;
}
DI void poll(const char* addr, bool active, u32 nonce) {
#pragma unroll 1
  for (u32 spins = 0; spins < SPIN_CAP; ++spins) {
    u32 v = __hip_atomic_load((GAS u32*)addr, RLX_AGENT);
    if (__all(!active || v == nonce)) break;
    __builtin_amdgcn_s_sleep(1);
  }
  asm volatile("" ::: "memory");
}
DI void poll16(const char* base, u32 off, u64 mask, u32 nonce) {
#pragma unroll 1
  for (u32 spins = 0; spins < SPIN_CAP; ++spins) {
    u32 v = nonce;
    asm volatile("s_mov_b64 exec, %3\n\tglobal_load_dword %0, %1, %2 sc1\n\ts_mov_b64 exec, -1\n\ts_waitcnt vmcnt(0)" : "+v"(v) : "v"(off), "s"(base), "s"(mask) : "memory");
    if (__all(v == nonce)) break;
    __builtin_amdgcn_s_sleep(PSLEEP);
  }
  __builtin_amdgcn_s_waitcnt(0x0F70);
  asm volatile("" ::: "memory");
}

DI void nonce_issue(const Params& p) {
  asm volatile("s_mov_b32 m0, %2\n\ts_mov_b64 exec, 1\n\tglobal_load_lds_dword %0, %1 sc1\n\ts_mov_b64 exec, -1" ::"v"(WS_NONCE), "s"(p.ws), "s"(L_NONCE) : "memory");
}
template <int NY> DI void stats_finish(const Params& p, u32 srow, int w, int lane) {
  wait_vm<NY>();
  struct { u32x4 xa; u32 xb; } r;
  r.xa = *(const LDS3 u32x4*)(size_t)((w + (w & 4)) * 1024 + lane * 16);
  r.xb = *(const LDS3 u32*)(size_t)(L_ROWB + w * 256 + lane * 4);
  const float x0 = blo(__builtin_amdgcn_readfirstlane(r.xa[0]));
  f32x2 s = {0.f, 0.f}, q = {0.f, 0.f};
#pragma unroll
  for (int i = 0; i < 5; i++) {
    const u32 u = i < 4 ? r.xa[i & 3] : r.xb;
    const f32x2 d = f32x2{blo(u), bhi(u)} - splat(x0);
    s += d; q = fma2(d, d, q);
  }
  const float m0 = sum_wave(s[0] + s[1]) * (1.f / 640.f), m2 = sum_wave(q[0] + q[1]) * (1.f / 640.f);
  const float mean = x0 + m0, rs = __builtin_amdgcn_rsqf(__builtin_fmaxf(__builtin_fmaf(-m0, m0, m2), 0.f) + 1e-5f);
  asm volatile("s_mov_b64 exec, 1\n\tglobal_store_dwordx2 %0, %1, %2 sc1\n\ts_mov_b64 exec, -1" ::"v"(WS_STATS + srow * 8), "v"(f32x2{mean, rs}), "s"(p.ws) : "memory");
}
struct Retile { const char* src; u32 dst[4], nat; };
DI void retile_setup(Retile& t, const Params& p, int b, int w) {
  const u32 uid = UID, ks = (uid * 6554) >> 16, line = uid - 10 * ks;
  const char* wo_ = p.wo - 54 * (32 * 1280);
  t.src = opaque_p((ks < 54 ? p.wd : wo_) + (ks * (32 * 1280) + line * 128));
#pragma unroll
  for (int i = 0; i < 4; i++) t.dst[i] = opaque_s0(WS_W2T + (5 * ks + 4 * line + i) * 1024 + (((4 * line + i) * 13) >> 6) * (W2T_STRIDE - 5 * 1024));
  const u32 sh = ks < 54 ? 31 : (ks - 54) & 31, nat = (0x21084u >> sh) & 1u;
  t.nat = opaque_s0(ks < 74 ? nat : 2u);
}
DI void retile_issue(const Retile& t, int w, int lane) {
  if (t.nat != 2) {
    const u32 ln = opaque(lane), vo = (ln >> 3) * 1280 + (((ln & 7) ^ ((ln >> 3) & 6)) * 16), vp = vo ^ 64, l = L_MISC + (w - 2) * 4096;
    glds<0, 4>(vo, t.src, l); glds<1024, 4>(vp + 8 * 1280, t.src, l); glds<2048, 4>(vo + 16 * 1280, t.src, l); glds<3072, 4>(vp + 24 * 1280, t.src, l);
  }
}
DI void retile_addr(const Retile& t, int w, u32 l16, u32& a0, u32& a1, u32& vo) {
  const u32 l = opaque(l16), n1 = t.nat & 1;
  const u32 rb = ((l & 768) << (1 + n1)) + (l & 192) * 2;
  a0 = opaque(L_MISC + (w - 2) * 4096 + rb + (((rb >> 3) & 96) ^ ((rb >> 4) & 64)) + (l & 48) / 2);
  a1 = opaque((a0 + (n1 ? 4 : 16) * 128) ^ (n1 * 64));
  vo = opaque(l | ((t.nat >> 1) << 31));
}

DI void flags_fetch(u32& f, const Params& p, int w, u32 l16) {
  asm volatile("v_and_b32 %0, 0x1f0, %1\n\tv_lshlrev_b32 %0, 2, %0\n\tglobal_load_dword %0, %0, %2 sc1" : "=&v"(f) : "v"(l16), "s"(p.ws + (WS_F0 + w * 2048)) : "memory");
}
DI void flags_check(u32 f, const Params& p, int w, u32 l16, u32 nonce STP) {
  asm volatile("" : "+v"(f)::"memory");
  if (__builtin_expect(!__all(f == nonce), 0)) { poll(p.ws + (WS_F0 + w * 2048 + (opaque(l16) & 0x1f0) * 4), true, nonce); STAMP(15); }
  asm volatile("" ::: "memory");
}
DI void stats_fetch(const Params& p, int w, u32 l16, int row0) {
  asm volatile("s_mov_b32 m0, %2\n\ts_mov_b64 exec, 0xffffff\n\tglobal_load_lds_dwordx4 %0, %1 sc1\n\ts_mov_b64 exec, -1"
               ::"v"(l16), "s"(p.ws + (WS_STATS + row0 * 8 + w * 384)), "s"(L_STATS + w * 384) : "memory");
}
template <int N, int E, int C> DI void sync_vm2(u32 c) {
  __builtin_amdgcn_s_waitcnt(0xC07F);
  asm volatile("s_cmp_eq_u32 %0, %3\n\ts_cbranch_scc1 .Lw%=\n\ts_waitcnt vmcnt(%1)\n.Lw%=:\n\ts_waitcnt vmcnt(%2) lgkmcnt(0)\n\ts_barrier"
               ::"s"(c), "i"(N), "i"(N + E), "i"(C) : "memory", "scc");
}
template <int IMM> DI void set_m0(u32 lds) { asm volatile("s_add_u32 m0, %0, %1" ::"s"(lds), "i"(IMM) : "memory", "scc"); }
template <int OFF, int K = 2> DI void glds_go(u32 voff, const char* sbase) {
  if constexpr (K == 1) asm volatile("global_load_lds_dwordx4 %0, %1 offset:%2 " WAUX ::"v"(voff), "s"(sbase), "i"(OFF) : "memory");
  else asm volatile("global_load_lds_dwordx4 %0, %1 offset:%2 " XAUX ::"v"(voff), "s"(sbase), "i"(OFF) : "memory");
}
template <int OFF> DI void glds_gom(u32 voff, const char* sbase, u64 mask) {
  asm volatile("s_mov_b64 exec, %3\n\tglobal_load_lds_dwordx4 %0, %1 offset:%2 " XAUX "\n\ts_mov_b64 exec, -1" ::"v"(voff), "s"(sbase), "i"(OFF), "s"(mask) : "memory");
}
DI void glds_step(u32& voff, u32 step, const char* sbase) {
  asm volatile("v_add_u32 %0, %1, %0\n\tglobal_load_lds_dwordx4 %0, %2 " WAUX : "+v"(voff) : "s"(step), "s"(sbase) : "memory");
}
DI void glds_lin(u32 l16, const char* sbase) {
  u32 t;
  asm volatile("v_add_u32 %0, m0, %1\n\tglobal_load_lds_dwordx4 %0, %2 sc1" : "=&v"(t) : "v"(l16), "s"(sbase) : "memory");
}
template <bool FIRST> DI void mfma_c(f32x4& c, bf16x8 a, bf16x8 b) {
  if constexpr (FIRST) asm volatile("v_mfma_f32_16x16x32_bf16 %0, %1, %2, 0" : "=&v"(c) : "v"(a), "v"(b));
  else mfma_ip(c, a, b);
}
template <int V> struct IC { static constexpr int v = V; };
template <int A, int B, class F> DI void static_for(F&& f) {
  if constexpr (A < B) { f(IC<A>{}); static_for<A + 1, B>(f); }
}
DI float sel4(u32 l16, f32x4 a, f32x4 b, f32x4 c, f32x4 d) {
  const float x = l16 < 256 ? a[0] : b[0], y = l16 < 768 ? c[0] : d[0];
  return l16 < 512 ? x : y;
}
template <int SLEEP> DI void lds_wait(u32 a, u32 n) {
  u32 t;
  asm volatile("s_branch .Lq%=\n.Lp%=:\n\ts_sleep %3\n.Lq%=:\n\tds_read_b32 %0, %1\n\ts_waitcnt lgkmcnt(0)\n\tv_cmp_gt_u32 vcc, %2, %0\n\ts_nop 0\n\ts_cbranch_vccnz .Lp%="
               : "=&v"(t) : "v"(a), "s"(n), "i"(SLEEP) : "memory", "vcc");
}
DI void put_sums(u32 a, f32x4 s0, f32x4 s1, f32x4 s2, f32x4 s3, u64 mask) {
  asm volatile("s_mov_b64 exec, %5\n\tds_write_b128 %0, %1\n\tds_write_b128 %0, %2 offset:64\n\tds_write_b128 %0, %3 offset:128\n\tds_write_b128 %0, %4 offset:192\n\ts_mov_b64 exec, -1"
               ::"v"(a), "v"(s0), "v"(s1), "v"(s2), "v"(s3), "s"(mask) : "memory");
}
DI u64 opaque_m(u64 v) { asm volatile("" : "+s"(v)); return v; }
DI u32 vadd(u32 v, u32 s) { asm volatile("v_add_u32 %0, %1, %0" : "+v"(v) : "s"(s)); return v; }
DI void pin5(u32& a, u32& b, u32& c, u32& d, u32& e) { asm volatile("" : "+v"(a), "+v"(b), "+v"(c), "+v"(d), "+v"(e)); }
DI u32 woff(int w, int lane, u32 rstride) {
  return (8 * (w >> 1) + (lane >> 3)) * rstride + ((((lane & 7) >> 1) ^ (((lane >> 4) & 1) | (w & 2))) * 32) + (lane & 1) * 16;
}
#define NOP (void)0
#define EPRIO(u) do { if ((u) == 0) SETPRIO(3); if ((u) == 1) SETPRIO(2); if ((u) == 3) SETPRIO(1); if ((u) == 4) SETPRIO(0); } while (0)
#define KPRIO(it) do { if (((it) & 1) == 0) SETPRIO(3 - (it) / 2); } while (0)
template <int I> DI void ldx(bf16x8& d, u32 l16, const char* pb) {
  asm volatile("global_load_dwordx4 %0, %1, %2 offset:%3 sc1" : "=v"(d) : "v"(l16), "s"(pb + ((I + 4) >> 3) * 8192), "i"((((I + 4) & 7) - 4) * 1024) : "memory");
}
#define LDX(i_) ldx<(i_)>(BR[i_], l16, pb)
#define EX(iv, n) do { if constexpr (C >= 8 && (n) < EXN) LDX(24 - (EXN * (2 * (C - 8) + (iv)) + (n))); } while (0)
#define EK(iv, n) do { if constexpr (C == 9 && (n) < 2 * EXN) LDX(24 - (2 * EXN * (iv) + (n))); } while (0)
DI void touch_w(u32& tw, u32 step, const char* base) {
  asm volatile("s_mov_b32 m0, %3\n\ts_mov_b64 exec, 0xff\n\tglobal_load_lds_dword %0, %2\n\ts_mov_b64 exec, -1\n\tv_add_u32 %0, %1, %0"
               : "+v"(tw) : "s"(step), "s"(base), "i"(L_JUNK) : "memory");
}
#define TWE(c) ((c) == TW1 || (c) == TW2)
#define TOUCH_TLB() do { if (w == 1) { const u32 o_ = (((b >> 3) * 11 + (opaque(lane) & 15)) << 16); glds4(o_ < WS_END ? o_ : 0u, p.ws, L_JUNK); \
    const u32 o2_ = (((b >> 3) + (opaque(lane) & 1) * 24) << 16); glds4(o2_ < 2048 * 1280 ? o2_ : 0u, p.out, L_JUNK); } } while (0)
#define RARE(c) __builtin_expect(!!(c), 0)

struct Pro { const char *sw0, *sxa, *st; u32 vw, v0, va[3], l16, tw, srow, kind; u64 xmask; };
struct Late { const char *w2t, *pb, *f1; u32 eo, nonce; };

#define acc(u, t) R[(u) * 4 + (t)]
#define cs(t) R[24 + (t)]
template <int NU, class LateF>
DI void ffn_loop(const Params& p, __amdgpu_buffer_rsrc_t rws, int b, int w, int lane, int ustart, u32 rstride, Pro& pr, Late& lt, LateF&& late,
                 f32x4 (&R)[28], bf16x8 (&BR)[25] STP) {
  constexpr int WWP = 16384, XA = 32768, XAP = 24576, XB = 81920, XBP = 20480;
  constexpr int NBX = NU - 3, NV = 4 + NBX + 1;
  const int rg = w >> 1, cg = w & 1;
  const char *sw = pr.sw0, *sxa = pr.sxa, *sxb = pr.sxa + 48 * 1280, *&w2t = lt.w2t, *&pb = lt.pb;
  u32(&va)[3] = pr.va, &vw = pr.vw;
  const u32 l16 = pr.l16, dw = 32 * rstride;
  const u32 la = XA + rg * 6144 + cg * 1024, lw = cg * 8192 + rg * 1024, dba = XB - XA - (rg >> 1) * ((rg & 1) + 1) * 2048, lb = la + dba;
  Retile rtl;
  u32 own;
#define SA(c, n) set_m0<((c) & 1) * XAP + (n) * 2048 - ((c) < 10 ? (c) * 128 : 0)>(la)
#define GA(c, n) do { if constexpr ((c) >= 10) glds_lin(l16, w2t); else if constexpr (NU == 6 && (n) == 0) glds_gom<(c) * 128>(pr.v0, sxa, pr.xmask); else glds_go<(c) * 128>(va[n], sxa); } while (0)
#define SX(c, n) do { if constexpr ((n) < NBX) set_m0<((c) & 1) * XBP + (n) * 2048 - ((c) < 10 ? (c) * 128 : 0)>(lb); } while (0)
#define GX(c, n) do { if constexpr ((n) < NBX) { if constexpr ((c) < 10) glds_go<(c) * 128>(va[n], sxb); else glds_lin(l16, w2t); } } while (0)
#define SW(c, ks) set_m0<((c) & 1) * WWP + (ks) * 4096>(lw)
#define GW(c) do { if constexpr ((c) < 10) glds_step(vw, dw, sw); else glds_lin(l16, w2t); } while (0)
#define PAD() asm volatile("s_nop 0" ::: "memory")
  SA(0, 2); va[2] = opaque(va[0] + 32 * 1280); PAD(); GA(0, 2);
  SB();
  const u32 xa0 = opaque(XA + rg * 6144 + (lane & 15) * 128 + (((lane >> 4) ^ ((lane & 15) >> 1)) * 16));
  const u32 xa1 = opaque(xa0 ^ 64), xb0 = opaque(xa0 + dba), xb1 = opaque(xb0 ^ 64);
  const int q = lane >> 4, qq = (lane >> 2) & 3, pp = lane & 3, ff = (qq >> 1) | ((q & 1) << 1);
  const u32 wt0 = opaque((8 * q + qq) * 128 + (((2 * cg) ^ ff) * 32) + pp * 8), wt1 = opaque(wt0 ^ 32);
  const u32 wu0 = opaque(wt0 + L_MISC), wu1 = opaque(wt1 + L_MISC);
  if constexpr (NU == 6) {
    if (~pr.xmask) {
      const u32 o = ((l16 >> 7) + 8 * cg) == (pr.kind ? 0 : 15) ? 0x3f803f80u : 0u;
      asm volatile("s_mov_b64 exec, %2\n\tds_write_b128 %0, %1\n\tds_write_b128 %0, %1 offset:%3\n\ts_mov_b64 exec, -1"
                   ::"v"(la + l16), "v"(u32x4{o, o, o, o}), "s"(~pr.xmask), "i"(XAP) : "memory");
    }
  }
  vw = opaque(vw + dw);
  SB();
  STAMP(27);
  stats_finish<5 + (TPOS < 2)>(p, pr.srow, w, lane);
  STAMP(1);
  SX(0, 0); PAD(); GX(0, 0); SX(0, 1); PAD(); GX(0, 1); SX(0, 2); PAD(); GX(0, 2);
  SW(1, 0); GW(1);
#define WA(n, off) (((n) & 1 ? wt1 : wt0) + (off) + (((n) >> 1) & 1) * 8192 + ((n) >> 2) * 4096)
#define LDW(c, n) do { if constexpr ((c) < 10) W[(c) & 1][(n) >> 2][(n) & 3] = ldtr(WA(n, ((c) & 1) * WWP), WA(n, ((c) & 1) * WWP) + 512); } while (0)
#define RX(c, u, k) do { if constexpr ((c) < 10) X[((c) * NU + (u)) & 1][k] = (u) < 3 ? lds128(((k) ? xa1 : xa0) + ((c) & 1) * XAP + (u) * 2048) \
                                                                                    : lds128(((k) ? xb1 : xb0) + ((c) & 1) * XBP + ((u) - 3) * 2048); } while (0)
  bf16x8 W[2][2][4], X[2][2], rf[4];
  u32 ca, nv, fl, ra0, ra1, rvo;
  sync_vm<NBX + 2>();
  RX(0, 0, 0); SB();
#pragma unroll
  for (int n = 0; n < 4; n++) { W[0][0][n] = ldtr((n & 1 ? wu1 : wu0) + (n >> 1) * 8192, (n & 1 ? wu1 : wu0) + (n >> 1) * 8192 + 512); SB(); }
  RX(0, 0, 1); SB();
  LDW(0, 4); SB(); LDW(0, 5); SB(); LDW(0, 6); SB(); LDW(0, 7); SB();
  if constexpr (TPOS == 2) touch_w(pr.tw, 6 * dw, pr.st);
  STAMP(2);

#define M1(u, n) mfma_c<(C == 0 && (n) < 4)>(acc(u, (n) & 3), W[P][(n) >> 2][(n) & 3], X[(C * NU + (u)) & 1][(n) >> 2])
#define STEP(u, f0, f1, f2, f3, f4, f5, f6, f7)                                                               \
  do { M1(u, 0); SB(); f0; SB(); M1(u, 1); SB(); f1; SB(); M1(u, 2); SB(); f2; SB(); M1(u, 3); SB(); f3; SB(); \
       M1(u, 4); SB(); f4; SB(); M1(u, 5); SB(); f5; SB(); M1(u, 6); SB(); f6; SB(); M1(u, 7); SB(); f7; SB(); } while (0)
#define TWO(a, b) do { a; b; } while (0)
#define AT(c, x) do { if constexpr (C == (c)) { x; } } while (0)
#define RR(c, i) do { if constexpr (NU == 5 && C == (c) + RT0) rf[i] = ldtr(ra0 ^ ((i) * 32), ra1 ^ ((i) * 32)); } while (0)
#define RS(c, i) do { if constexpr (NU == 5 && C == (c) + RT0) st16<16>(__builtin_bit_cast(u32x4, rf[i]), rws, rvo, rtl.dst[i]); } while (0)
#define SP(k) do { if constexpr (C == 3 && (k) == 0) nv = *(const volatile LDS3 u32*)(size_t)L_NONCE;          \
                   if constexpr (C == 3 && (k) == 3) lt.nonce = opaque_s0(__builtin_amdgcn_readfirstlane(nv) + 1u); \
                   if constexpr ((C == 4 || C == 5) && ((k) & 1) == 0) late(IC<2 * (C - 4) + (k) / 2>{}); } while (0)
  static_for<0, 10>([&](auto cc) {
    constexpr int C = cc.v, P = C & 1;
    SETPRIO(1);
    if constexpr (C == 0) STEP(0, RX(C, 1, 0), TWO(RX(C, 1, 1), SA(1, 0)), TWO(GA(1, 0), SA(1, 1)), TWO(GA(1, 1), SA(1, 2)), TWO(GA(1, 2), SW(1, 1)), TWO(GW(1), SX(1, 0)), TWO(GX(1, 0), SX(1, 1)), GX(1, 1));
    else STEP(0, RX(C, 1, 0), RX(C, 1, 1), SX(C + 1, 1), GX(C + 1, 1), SP(0), TWO(SP(1), RR(3, 3)), TWO(SP(2), RS(3, 2)), SP(3));
    SETPRIO(0); STEP(1, RX(C, 2, 0), TWO(RX(C, 2, 1), SX(C + 1, 2)), GX(C + 1, 2), EX(0, 0), TWO(EX(0, 3), SW(C + 2, 0)), GW(C + 2), EX(0, 1), TWO(EX(0, 2), RS(3, 3)));
    if constexpr (NU == 5 && C == 1 + RT0) sync_vm2<NV + TWE(C - 1), 4, 0>(own);
    else sync_vm<NV + TWE(C - 1) + (C == 0 && TPOS == 2) + (NU == 5 && C == 3 + RT0 ? 4 : C == 7 ? 1 : C == 8 ? 1 + EXN : C == 9 ? 1 + 3 * EXN : 0)>();
    SB();
    auto hook_y = [&]() {
      if constexpr (NU == 5 && C == 0) { retile_setup(rtl, p, b, w); own = rtl.nat >> 1; }
      if constexpr (NU == 5 && C == 2 + RT0) retile_addr(rtl, w, l16, ra0, ra1, rvo);
      if constexpr (NU == 6 && C == 4 + RT0) { if (w == 0) st_flag(p.ws + WS_F0 + b * 64, lt.nonce); }
      if constexpr (C == 9) {
        ca = opaque(L_CS + cg * 256 + ((opaque(l16) >> 4) & 48));
        if constexpr (NU == 6) { if (rg == 0) put_sums(ca, acc(0, 0), acc(0, 1), acc(0, 2), acc(0, 3), pr.kind ? 0x0001000100010001ull : 0x8000800080008000ull); }
      }
      SB();
    };
    if constexpr (NU == 6) {
      SETPRIO(2); STEP(2, RX(C, 3, 0), RX(C, 3, 1), LDW(C + 1, 0), TWO(LDW(C + 1, 1), SA(C + 2, 0)), GA(C + 2, 0), NOP, SW(C + 2, 1), GW(C + 2));
      hook_y();
      SETPRIO(1); STEP(3, RX(C, 4, 0), RX(C, 4, 1), TWO(LDW(C + 1, 2), EX(1, 0)), SA(C + 2, 1), GA(C + 2, 1), NOP, EX(1, 1), NOP);
      SETPRIO(0); STEP(4, RX(C, 5, 0), RX(C, 5, 1), TWO(LDW(C + 1, 3), EX(1, 2)), SA(C + 2, 2), GA(C + 2, 2), NOP, EX(1, 3), NOP);
    } else {
      hook_y();
      SETPRIO(1); STEP(2, RX(C, 3, 0), RX(C, 3, 1), TWO(LDW(C + 1, 0), EX(1, 0)), TWO(LDW(C + 1, 1), SA(C + 2, 0)),
                       GA(C + 2, 0), SW(C + 2, 1), GW(C + 2), TWO(EX(1, 3), RR(2, 0)));
      SETPRIO(0); STEP(3, TWO(RX(C, 4, 0), EX(1, 1)), RX(C, 4, 1), TWO(LDW(C + 1, 2), EX(1, 2)), TWO(LDW(C + 1, 3), SA(C + 2, 1)),
                       TWO(GA(C + 2, 1), RR(2, 1)), SA(C + 2, 2), GA(C + 2, 2), RS(2, 0));
    }
    if constexpr (C == 7) { wait_vm<NV>(); flags_check(fl, p, w, l16, lt.nonce STA); }
    if constexpr (C == 9) sync_vm<63>();
    else if constexpr (NU == 5 && C == 1 + RT0) sync_vm2<NV + TWE(C - 1), 4, 0>(own);
    else sync_vm<NV + TWE(C - 1) + (NU == 5 && C == 2 + RT0 ? 1 : NU == 5 && C == 3 + RT0 ? 4 : C == 8 ? 1 + 2 * EXN : 0)>();
    SB();
    LSTAMP(16 + C);
    SETPRIO(2);
    if constexpr (C < 9) STEP(NU - 1, RX(C + 1, 0, 0), RX(C + 1, 0, 1), LDW(C + 1, 4), TWO(TWO(LDW(C + 1, 5), AT(6, flags_fetch(fl, p, w, l16))), AT(7, stats_fetch(p, w, l16, ustart * 16))), LDW(C + 1, 6), TWO(LDW(C + 1, 7), SX(C + 2, 0)), TWO(GX(C + 2, 0), RR(2, 2)), RS(2, 1));
    else STEP(NU - 1, TWO(cs(0) = ldsf4(ca), SX(11, 0)), TWO(cs(1) = ldsf4(ca + 64), GX(11, 0)), TWO(cs(2) = ldsf4(ca + 128), SX(11, 1)),
              TWO(cs(3) = ldsf4(ca + 192), GX(11, 1)), SX(11, 2), GX(11, 2), NOP, NOP);
    if constexpr (C == RT0) { if constexpr (NU == 5) retile_issue(rtl, w, lane); else TOUCH_TLB(); }
    if constexpr (C == 0 && NU == 6) { if (w == 0) nonce_issue(p); }
    if constexpr (TWE(C)) touch_w(pr.tw, 6 * dw, pr.st);
    if constexpr (NU == 6 && C == 8) { if ((b | w) == 0) st_flag(p.ws + WS_NONCE, lt.nonce); }
    SB();
  });
  SETPRIO(0);
#undef SA
#undef GA
#undef SX
#undef GX
#undef SW
#undef GW
#undef WA
#undef LDW
#undef RX
#undef RR
#undef RS
#undef M1
#undef STEP
}

DI void ffn_epilogue(__amdgpu_buffer_rsrc_t rws, int w, u32 v16, u32 role, u32 ub, u32 kind, u32 so, int strip, const char* w2t, const char* pb, f32x4 (&R)[28], bf16x8 (&BR)[25]) {
  f32x2 st[6];
  const u32 a2 = opaque(L_STATS + ub * 128 + ((v16 >> 1) & 120));
#pragma unroll
  for (int u = 1; u < 6; u++) st[u] = *(const LDS3 f32x2*)(size_t)(a2 + u * 128);
  const u32 d0 = (kind | ub) ? 0 : 22, keep = ub ? 3 : kind == 2 ? 0 : kind + 1;
  st[0] = *(const LDS3 f32x2*)(size_t)(a2 + d0 * 128);
  const u32 v0 = opaque((keep >> ((v16 >> 7) & 1)) & 1 ? v16 : 0x80000000u);
#define EPI_HEAD(u)                                                                                           \
  if (u == 5 && role != 0) break;                                                                             \
  EPRIO(u);                                                                                                   \
  if (u < 5 && opaque_s0(w ^ SWAP) >= 4)                                                                      \
    static_for<0, 25 - NEX>([&](auto i) { if (i.v >= (25 - NEX) * u / 5 && i.v < (25 - NEX) * (u + 1) / 5) ldx<i.v>(BR[i.v], v16, pb); }); \
  if (u < 5 && opaque_s0(w ^ SWAP) < 4) { asm volatile("s_mov_b32 m0, %0" ::"s"(122880 + (w & 3) * 1024 + u * 4096) : "memory"); glds_lin(v16, w2t); \
    if (u < 3) { asm volatile("s_mov_b32 m0, %0" ::"s"(143360 + (w & 3) * 1024 + u * 4096) : "memory"); glds_lin(v16, w2t); } }
#define H(x, n) half(x, (n) & 1)
  if (strip < 27) {
#pragma unroll
    for (int u = 0; u < 6; u++) {
      EPI_HEAD(u)
      const float rs = st[u][1];
      const f32x2 nm = splat(-st[u][0]), c1 = splat(rs * -1.44269504f), cv = splat(__builtin_amdgcn_rcpf(rs * rs));
      f32x2 g[4], a[4], up[4];
      u32x4 o;
#pragma unroll
      for (int n = 0; n < 4; n++) g[n] = fma2(nm, H(cs(n >> 1), n), H(acc(u, n >> 1), n));
      SB();
#pragma unroll
      for (int n = 0; n < 4; n++) a[n] = g[n] * c1;
      SB();
#pragma unroll
      for (int n = 0; n < 4; n++) {
        up[n] = fma2(nm, H(cs(2 + (n >> 1)), n), H(acc(u, 2 + (n >> 1)), n));
        a[n] = f32x2{__builtin_amdgcn_exp2f(a[n][0]), __builtin_amdgcn_exp2f(a[n][1])};
        SB();
      }
#pragma unroll
      for (int n = 0; n < 4; n++) a[n] = fma2(a[n], cv, cv);
      SB();
#pragma unroll
      for (int n = 0; n < 4; n++) {
        g[n] *= up[n];
        a[n] = f32x2{__builtin_amdgcn_rcpf(a[n][0]), __builtin_amdgcn_rcpf(a[n][1])};
        SB();
      }
#pragma unroll
      for (int n = 0; n < 4; n++) g[n] *= a[n];
      SB();
#pragma unroll
      for (int n = 0; n < 4; n++) o[n] = pk2(g[n][0], g[n][1]);
      st16<16>(o, rws, u ? v16 : v0, so + (u ? u : d0) * (74 * 1024));
    }
#pragma unroll
    for (int i = 0; i < 28; i++) asm volatile("" : "=v"(R[i]));
  }
  asm volatile("; q boundary");
  if (opaque_s0(strip) >= 27) {
#pragma unroll
    for (int u = 0; u < 6; u++) {
      if (u == 5 && role != 0) break;
      EPRIO(u);
      if (u < 5 && (QALL || opaque_s0(w) >= 4)) static_for<NK, NK + 10>([&](auto i) { if ((i.v - NK) / 2 == u) ldx<i.v>(BR[i.v], v16, pb); });
      const f32x2 nm = splat(-st[u][0]), c1 = splat(st[u][1] * (QRAW ? 1.f : 1.44269504f));
#pragma unroll
      for (int m = 0; m < 2; m++) {
        f32x2 a[4], e[4];
        u32x4 o;
#pragma unroll
        for (int n = 0; n < 4; n++) a[n] = fma2(nm, H(cs(2 * m + (n >> 1)), n), H(acc(u, 2 * m + (n >> 1)), n));
        SB();
#pragma unroll
        for (int n = 0; n < 4; n++) a[n] *= c1;
        SB();
#pragma unroll
        for (int n = 0; n < 4; n++) e[n] = exp2c(a[n]);
        SB();
#pragma unroll
        for (int n = 0; n < 4; n++) a[n] = phi2(a[n], 1.44269504e-6f, e[n]);
        SB();
#pragma unroll
        for (int n = 0; n < 4; n++) o[n] = pk2(a[n][0], a[n][1]);
        st16<16>(o, rws, u ? v16 : v0, so + (u ? u : d0) * (20 * 1024) + m * 2048);
      }
    }
  }
#undef H
#undef EPI_HEAD
  SETPRIO(0);
}

#undef acc
#undef cs
#define acc(u, t) R[(u) * 5 + (t)]
#define cs(t) R[20 + (t)]
template <class LateF>
DI void kv_loop(const Params& p, int b, int w, int lane, int g, int rt8, Pro& pr, Late& lt, LateF&& late, f32x4 (&R)[28], bf16x8 (&BR)[25] STP) {
  constexpr int WWP = 32768, XA = 65536, XP = 16384, XB = 98304;
  constexpr int NV = 8;
  const int rg = w >> 1, cg = w & 1;
  const char *sw = pr.sw0, *sxa = pr.sxa, *sxb = pr.sxa + 32 * 1280, *&w2t = lt.w2t, *&pb = lt.pb;
  u32(&va)[3] = pr.va, &vw = pr.vw;
  const u32 l16 = pr.l16, dw = opaque_s0(32 * 3840);
  const u32 la = XA + rg * 4096 + cg * 1024, lw = cg * 16384 + rg * 1024;
  const u64 lo4 = 0x0f0f0f0f0f0f0f0full, wmk = ((g >> 1) ^ rg) & 1 ? ~lo4 : lo4;
  const u64 wm0 = opaque_m(g & 2 ? wmk : ~0ull), wm1 = opaque_m(g & 2 ? ~0ull : wmk);
#define SA(c, n) do { if constexpr ((c) < 11) set_m0<((c) & 1) * XP + (n) * 2048 - ((c) < 10 ? (c) * 128 : 0)>(la); } while (0)
#define GA(c, n) do { if constexpr ((c) < 11) do { if constexpr ((c) < 10) glds_go<(c) * 128>(va[n], sxa); else glds_lin(l16, w2t); } while (0); } while (0)
#define SX(c, n) do { if constexpr ((c) < 11) set_m0<XB - XA + ((c) & 1) * XP + (n) * 2048 - ((c) < 10 ? (c) * 128 : 0)>(la); } while (0)
#define GX(c, n) do { if constexpr ((c) < 11) do { if constexpr ((c) < 10) glds_go<(c) * 128>(va[n], sxb); else glds_lin(l16, w2t); } while (0); } while (0)
#define SW(c, ks, m) do { if constexpr ((c) < 11) set_m0<((c) & 1) * WWP + (ks) * 4096 + (m) * ((c) < 10 ? 8192 - 128 : 8192)>(lw); } while (0)
#define GW(c, m) do { if constexpr ((c) < 11) do { if constexpr ((c) >= 10) glds_lin(l16, w2t); else if constexpr (!KMASK) { if constexpr (m) glds_go<128, 1>(vw, sw); else glds_step(vw, dw, sw); } \
    else if constexpr (m) glds_gom<128>(vw, sw, wm1); else { vw = vadd(vw, dw); glds_gom<0>(vw, sw, wm0); } } while (0); } while (0)
  SW(0, 1, 0); GW(0, 0); SW(0, 1, 1); PAD(); GW(0, 1);
  SB();
  const u32 xa0 = opaque(XA + rg * 4096 + (lane & 15) * 128 + (((lane >> 4) ^ ((lane & 15) >> 1)) * 16)), xa1 = opaque(xa0 ^ 64);
  u32 wt[3], wtt[3];
  {
    const int q = lane >> 4, qq = (lane >> 2) & 3, pp = lane & 3, ff = (qq >> 1) | ((q & 1) << 1);
#pragma unroll
    for (int n = 0; n < 3; n++) {
      const int cb = ((160 * g) & 127) + 80 * cg + (n == 0 ? 8 * pp : n == 1 ? 40 + 8 * pp : 32 + 40 * (pp & 1));
      wt[n] = opaque(((n == 2 ? (pp >> 1) * 2 : 0) + (cb >> 7)) * 8192 + (8 * q + qq) * 128 + ((((cb >> 5) & 3) ^ ff) * 32) + (cb & 31));
      wtt[n] = opaque(wt[n] + L_MISC);
    }
  }
  bf16x8 ones = {0x3f80, 0x3f80, 0x3f80, 0x3f80, 0x3f80, 0x3f80, 0x3f80, 0x3f80};
  asm volatile("" : "+v"(ones));
#pragma unroll
  for (int t = 0; t < 5; t++) { cs(t) = f32x4{0.f, 0.f, 0.f, 0.f}; pin(cs(t)); }
  SB();
  STAMP(27);
  stats_finish<6 + (TPOS < 2)>(p, pr.srow, w, lane);
  STAMP(1);
  SX(0, 0); PAD(); GX(0, 0); SX(0, 1); PAD(); GX(0, 1);
  SW(1, 0, 0); GW(1, 0); SW(1, 0, 1); PAD(); GW(1, 1);
#define WA(wt, off, t) (wt[(t) == 4 ? 2 : (t) & 1] + (off) + ((t) == 2 || (t) == 3 ? 16384 : 0))
#define RX(c, u, k) do { if constexpr ((c) < 10) X[(u) & 1][k] = lds128(((k) ? xa1 : xa0) + ((u) >> 1) * (XB - XA) + ((c) & 1) * XP + ((u) & 1) * 2048); } while (0)
#define RW(c, ks, t) do { if constexpr ((c) < 10) W[(c) & 1][ks][t] = ldtr(WA(wt, ((c) & 1) * WWP + (ks) * 4096, t), WA(wt, ((c) & 1) * WWP + (ks) * 4096, t) + 512); } while (0)
  bf16x8 W[2][2][5], X[2][2];
  u32 ca, nv, fl;
  sync_vm<5>();
  RX(0, 0, 0); SB();
#pragma unroll
  for (int t = 0; t < 5; t++) { W[0][0][t] = ldtr(WA(wtt, 0, t), WA(wtt, 0, t) + 512); SB(); }
  RX(0, 0, 1); SB();
  RW(0, 1, 0); SB(); RW(0, 1, 1); SB(); RW(0, 1, 2); SB(); RW(0, 1, 3); SB(); RW(0, 1, 4); SB();
  if constexpr (TPOS == 2) touch_w(pr.tw, 6 * dw, pr.st);
  STAMP(2);

#define M1(u, n) mfma_c<(C == 0 && (n) < 5)>(acc(u, (n) % 5), X[(u) & 1][(n) / 5], W[P][(n) / 5][(n) % 5])
#define STEP(u, f0, f1, f2, f3, f4, f5, f6, f7, f8, f9)                                                       \
  do { M1(u, 0); SB(); f0; SB(); M1(u, 1); SB(); f1; SB(); M1(u, 2); SB(); f2; SB(); M1(u, 3); SB(); f3; SB(); M1(u, 4); SB(); f4; SB(); \
       M1(u, 5); SB(); f5; SB(); M1(u, 6); SB(); f6; SB(); M1(u, 7); SB(); f7; SB(); M1(u, 8); SB(); f8; SB(); M1(u, 9); SB(); f9; SB(); } while (0)
#define CSUM(ks, G)                                                                                           \
  asm volatile("s_cmp_lg_u32 %11, %12\n\ts_cbranch_scc1 .Lcs%=\n\t"                                         \
               "v_mfma_f32_16x16x32_bf16 %0, %10, %5, %0\n\tv_mfma_f32_16x16x32_bf16 %1, %10, %6, %1\n\t"    \
               "v_mfma_f32_16x16x32_bf16 %2, %10, %7, %2\n\tv_mfma_f32_16x16x32_bf16 %3, %10, %8, %3\n\t"    \
               "v_mfma_f32_16x16x32_bf16 %4, %10, %9, %4\n.Lcs%=:"                                            \
               : "+v"(cs(0)), "+v"(cs(1)), "+v"(cs(2)), "+v"(cs(3)), "+v"(cs(4))                              \
               : "v"(W[P][ks][0]), "v"(W[P][ks][1]), "v"(W[P][ks][2]), "v"(W[P][ks][3]), "v"(W[P][ks][4]), "v"(ones), "s"(rg), "i"(G) : "scc")
  static_for<0, 10>([&](auto cc) {
    constexpr int C = cc.v, P = C & 1;
    CSUM(0, 2 * P);
    SB();
    SETPRIO(0);
    if constexpr (C == 0) STEP(0, TWO(RX(C, 1, 0), SA(1, 0)), TWO(TWO(RX(C, 1, 1), GA(1, 0)), SA(1, 1)), TWO(GA(1, 1), SW(1, 1, 0)), TWO(GW(1, 0), SW(1, 1, 1)), TWO(GW(1, 1), SX(1, 0)),
                               TWO(GX(1, 0), SX(1, 1)), TWO(GX(1, 1), SW(2, 0, 0)), TWO(GW(2, 0), SW(2, 0, 1)), GW(2, 1), NOP);
    else STEP(0, RX(C, 1, 0), TWO(RX(C, 1, 1), EK(0, 0)), SW(C + 2, 0, 0), GW(C + 2, 0), TWO(EK(0, 3), SP(0)), SW(C + 2, 0, 1), GW(C + 2, 1), TWO(EK(0, 1), SP(2)), SP(3), EK(0, 2));
    CSUM(1, 2 * P + 1);
    sync_vm<NV + TWE(C - 1) + (C == 0 && TPOS == 2) + (C == 7 || C == 8 ? 1 : C == 9 ? 2 * EXN : 0)>();
    SB();
    SETPRIO(1); STEP(1, RX(C, 2, 0), RX(C, 2, 1), RW(C + 1, 0, 0), RW(C + 1, 0, 1), TWO(RW(C + 1, 0, 2), SA(C + 2, 0)), GA(C + 2, 0), EK(1, 0), SW(C + 2, 1, 0), GW(C + 2, 0), EK(1, 3));
    if constexpr (C == 9) {
      const u32 lo = opaque(l16);
      *(LDS3 float*)(size_t)(L_CS + cg * 1280 + rg * 4 + lo) = sel4(lo, cs(0), cs(1), cs(2), cs(3));
      *(LDS3 float*)(size_t)(L_CS + cg * 1280 + rg * 4 + 1024 + (lo & 240)) = cs(4)[0];
      ca = opaque(L_CS + cg * 1280 + (lo & 240));
    }
    SB();
    SETPRIO(0); STEP(2, RX(C, 3, 0), RX(C, 3, 1), RW(C + 1, 0, 3), TWO(RW(C + 1, 0, 4), SW(C + 2, 1, 1)), GW(C + 2, 1), EK(1, 1), SA(C + 2, 1), GA(C + 2, 1), NOP, EK(1, 2));
    if constexpr (C == 7) { wait_vm<NV>(); flags_check(fl, p, w, l16, lt.nonce STA); }
    if constexpr (C == 9) sync_vm<63>();
    else sync_vm<NV + TWE(C - 1) + (C == 8)>();
    SB();
    LSTAMP(16 + C);
#define CL(t) cs(t) = ldsf4(ca + (t) * 256)
#define CA(t) cs(t)[0] = add1(add1(cs(t)[0], cs(t)[1]), add1(cs(t)[2], cs(t)[3]))
    SETPRIO(1);
    if constexpr (C == 9) STEP(3, TWO(CL(0), CL(1)), TWO(CL(2), CL(3)), CL(4), NOP, NOP, CA(0), CA(1), CA(2), CA(3), CA(4));
    else STEP(3, RX(C + 1, 0, 0), RX(C + 1, 0, 1), RW(C + 1, 1, 0), TWO(TWO(RW(C + 1, 1, 1), AT(6, flags_fetch(fl, p, w, l16))), AT(7, stats_fetch(p, w, l16, rt8 * 256))), RW(C + 1, 1, 2), RW(C + 1, 1, 3), TWO(RW(C + 1, 1, 4), SX(C + 2, 0)), GX(C + 2, 0), SX(C + 2, 1), GX(C + 2, 1));
    if constexpr (C == 0) { if (w == 0) nonce_issue(p); }
    if constexpr (TWE(C)) touch_w(pr.tw, 6 * dw, pr.st);
    if constexpr (C == 3) { if (w == 0) st_flag(p.ws + WS_F0 + b * 64, lt.nonce); }
    SB();
  });
  SETPRIO(0);
#undef SA
#undef GA
#undef SX
#undef GX
#undef SW
#undef GW
#undef WA
#undef RX
#undef RW
#undef M1
#undef STEP
#undef CSUM
#undef CL
#undef CA
}

DI void kv_epilogue(__amdgpu_buffer_rsrc_t rws, int w, u32 l16, int g, int rt8, const char* w2t, const char* pb, f32x4 (&R)[28], bf16x8 (&BR)[25]) {
  const u32 a2 = opaque(L_STATS + (w >> 1) * 512 + ((l16 >> 3) & 96));
  const bool isk = !(l16 & 128);
  const float lo4 = isk ? 1.44269504e-6f : -__builtin_inff();
  const f32x2 on4 = splat(isk ? 1.f : 0.f);
  f32x4 S[7], sv[8];
#pragma unroll
  for (int n = 0; n < 4; n++) sv[n] = ldsf4(a2 + (n >> 1) * 128 + (n & 1) * 16);
#pragma unroll
  for (int n = 0; n < 7; n++) S[n] = f32x4{0.f, 0.f, 0.f, 0.f};
  static_for<0, 2>([&](auto kc) {
    constexpr int kk = kc.v;
    u32x4 T[5];
    static_for<0, 4>([&](auto nc) {
      constexpr int n = nc.v, u = 2 * kk + (n >> 1), it = 4 * kk + n;
      KPRIO(it);
      if constexpr (it < 7) {
        set_m0<(it < 4 ? 32768 + it * 8192 : it < 6 ? 81920 + (it - 4) * 8192 : 114688)>(w * 1024); glds_lin(l16, w2t);
      }
      static_for<0, KE>([&](auto e) { if constexpr (24 - NEX - KE * (it + kk) - e.v >= 0) LDX(24 - NEX - KE * (it + kk) - e.v); });
      if constexpr (kk == 0 && n >= 2) { sv[2 * n] = ldsf4(a2 + n * 128); sv[2 * n + 1] = ldsf4(a2 + n * 128 + 16); }
      const f32x2 nmean = {-sv[it][0], -sv[it][2]}, rs = {sv[it][1], sv[it][3]}, c1 = rs * 1.44269504f;
      f32x2 y[5], e[3];
#pragma unroll
      for (int t = 0; t < 5; t++) y[t] = fma2(nmean, splat(cs(t)[0]), half(acc(u, t), n & 1));
      SB();
      y[0] *= c1; y[1] *= c1; y[4] *= c1; y[2] *= rs; y[3] *= rs;
      SB();
      e[2] = exp2c(y[4]); e[0] = exp2c(y[0]); e[1] = exp2c(y[1]);
      u32 t2 = pk2(y[2][0], y[2][1]), t3 = pk2(y[3][0], y[3][1]);
      SB();
      e[2] *= on4;
      y[0] = phi2(y[0], 1.44269504e-6f, e[0]); y[1] = phi2(y[1], 1.44269504e-6f, e[1]); y[4] = phi2(y[4], lo4, e[2]);
      SB();
      u32 t0 = pk2(y[0][0], y[0][1]), t1 = pk2(y[1][0], y[1][1]), t4 = pk2(y[4][0], y[4][1]);
      pin5(t0, t1, t2, t3, t4);
      T[0][n] = t0; T[1][n] = t1; T[2][n] = t2; T[3][n] = t3; T[4][n] = t4;
    });
    u32x4 T4a, T4b;
#pragma unroll
    for (int i = 0; i < 4; i++) {
      T4a[i] = __builtin_amdgcn_update_dpp(T[4][i], T[4][i], 0x128, 0xf, 0xc, false);
      T4b[i] = isk ? 0x3f803f80u : T[4][i];
    }
#define BC(v) __builtin_bit_cast(bf16x8, v)
    S[2] = mfma(BC(T[0]), BC(T4b), S[2]); S[3] = mfma(BC(T[1]), BC(T4b), S[3]);
    S[4] = mfma(BC(T4a), BC(T[2]), S[4]); S[5] = mfma(BC(T4a), BC(T[3]), S[5]); S[6] = mfma(BC(T4a), BC(T4b), S[6]);
    S[0] = mfma(BC(T[0]), BC(T[2]), S[0]); S[1] = mfma(BC(T[1]), BC(T[3]), S[1]);
    SB();
    static_for<0, KE>([&](auto e) { if constexpr (24 - NEX - KE * (4 + 5 * kk) - e.v >= 0) LDX(24 - NEX - KE * (4 + 5 * kk) - e.v); });
  });
  {
    const u32 a = opaque(L_RED + w * 4096 + l16);
    f32x4 m2, m3;
#pragma unroll
    for (int i = 0; i < 4; i++) { m2[i] = (l16 & 64) ? S[3][i] : S[2][i]; const float x = l16 < 256 ? S[4][i] : S[5][i]; m3[i] = l16 < 512 ? x : S[6][i]; }
    *(LDS3 f32x4*)(size_t)a = S[0]; *(LDS3 f32x4*)(size_t)(a + 1024) = S[1]; *(LDS3 f32x4*)(size_t)(a + 2048) = m2; *(LDS3 f32x4*)(size_t)(a + 3072) = m3;
  }
  const u32 a = opaque(L_RED + w * 2048 + 2 * l16), so = opaque_s0(WS_KVP + (g * 8 + rt8) * KVP_STRIDE + w * 1024);
  sync_lds();
  if (w < 4) {
    const f32x4 lo = (ldsf4(a) + ldsf4(a + 8192)) + (ldsf4(a + 16384) + ldsf4(a + 24576));
    const f32x4 hi = (ldsf4(a + 16) + ldsf4(a + 8208)) + (ldsf4(a + 16400) + ldsf4(a + 24592));
    st16<16>(u32x4{pk2(lo[0], lo[1]), pk2(lo[2], lo[3]), pk2(hi[0], hi[1]), pk2(hi[2], hi[3])}, rws, l16, so);
  }
}
#undef acc
#undef cs

constexpr int mstep(int i) { return i < 320 ? i / 20 : 16 + (i - 320) % 15 / 5; }
constexpr int munit(int i) { return i < 320 ? i % 20 / 5 : (i - 320) / 15; }
struct Stream { int e[128], upto[380], n; };
constexpr Stream make_stream() {
  Stream q{};
  q.upto[19] = WIN;
  for (int i = 20; i < 380; i++) {
    if (STYPE[mstep(i)] == 'S' && munit(i) == 0) q.e[q.n++] = mstep(i) * 16 + 4 + i % 5;
    if (i % 5 == 0) q.e[q.n++] = mstep(i) * 16 + munit(i);
    q.upto[i] = q.n + WIN;
  }
  return q;
}

typedef u64 u64x8 __attribute__((ext_vector_type(8)));
__global__ __launch_bounds__(512, 2) void fused_block(Params) {
  __shared__ __attribute__((aligned(16))) char smem[L_TOTAL];
  STAMP(0);
  u64x8 karg;
  asm volatile("s_load_dwordx16 %0, %1, 0x0" : "=s"(karg) : "s"(__builtin_amdgcn_kernarg_segment_ptr()), "s"((LDS3 char*)smem) : "memory");
  const int lane = threadIdx.x & 63;
  int b = opaque_s0(blockIdx.x), w = opaque_s(threadIdx.x >> 6);
  {
    u64 pc; u32 off = ((b & ~7) * 2 + 2 * w + lane) * 128;
    asm volatile("s_getpc_b64 %0\n.Lpc0:\n\ts_mov_b32 m0, %2\n\tv_min_u32 %1, .Lkend-.Lpc0-4, %1\n\ts_mov_b64 exec, 3\n\tglobal_load_lds_dword %1, %0\n\ts_mov_b64 exec, -1\n"
                 "\t.subsection 1\n.Lkend:\n\t.subsection 0"
                 : "=&s"(pc), "+v"(off) : "i"(L_JUNK) : "memory");
  }
  b = opaque_s0(b); w = opaque_s0(w);
  const int xcd = b & 7, idx = b >> 3, cg = w & 1, rg = w >> 1;
  const bool kv = b >= 192;
  const int i3 = (idx * 11) >> 5, strip = (xcd & 3) + 4 * i3, g = 2 * (xcd & 3) + ((idx >> 2) & 1);
  const u32 rstride = strip >= 27 ? 3840 : 3456, dw32 = 32 * rstride;
  const u32 wo_k = 1280 + cg * 1280 + ((160 * g) & ~127), wo_q = (strip - 27) * 256 + cg * 128, wo_f = strip * 128, wo_w = kv ? wo_k : wo_q;
  const u32 wo0 = opaque_s0(strip >= 27 ? wo_w : wo_f), wo1 = opaque_s0(kv ? 128 : dw32);
  const u32 lw0 = opaque_s0(cg * (kv ? 16384 : 8192) + rg * 1024), lw1 = opaque_s0(lw0 + (kv ? L_MISC + 8192 : 4096));
  Pro pr;
  pr.l16 = opaque(lane * 16);
  pr.vw = opaque(woff(w, lane, rstride));
  const u32 srow = opaque_s0(SROW), ro_ = opaque_s0(srow * 1280), rl_ = opaque_s0((w + (w & 4)) * 1024), l4_ = opaque(lane * 4 + 1024);
  SB();
  asm volatile("s_waitcnt lgkmcnt(0)" : "+s"(karg) : "s"(__builtin_amdgcn_kernarg_segment_ptr()));
  const Params p = {(const char*)karg[0], (const char*)karg[1], (const char*)karg[2], (const char*)karg[3], (const char*)karg[4], (const char*)karg[5], (char*)karg[6], (char*)karg[7]};
  glds<0, 3>(pr.l16, p.x + ro_, rl_);
  glds4(l4_, p.x + ro_, L_ROWB + w * 256);
  const char* wgu = cg ? p.wu : p.wg;
  pr.srow = srow;
  pr.st = strip >= 27 ? p.wqkv : wgu;
  pr.sw0 = pr.st + wo0;
  glds<L_MISC, 1>(pr.vw, pr.sw0, lw0);
  glds<0, 1>(pr.vw, pr.sw0 + wo1, lw1);
  SB();
  const int z2 = opaque_s0(0), rt = 3 * ((xcd >> 2) - i3) + idx + z2, rt8 = (xcd & 4) + (idx & 3) + z2;
  {
    const u32 jf = (strip * 6 + rt) * 4 + rg, jq = (strip - 27) * 6 + rt, jk = 30 + g * 8 + rt8, jw = (kv ? jk : jq) * 8 + w;
    const u32 j = strip >= 27 ? jw : jf, jm = j < 720 ? j : 719;
    pr.tw = opaque(2 * dw32 + jm * 1024 + (lane & 7) * 128);
  }
#define TOUCH_FIRST() touch_w(pr.tw, 6 * dw32, pr.st)
  TOUCH_FIRST();
  const int us0 = (int)(0x6b56402b1600ull >> ((8 * rt) & 63)) & 255, us1 = 16 * rt8 + 1, ustart = (kv ? us1 : us0) - 1;
  const u32 lx = (kv ? 65536 : 32768) + (rg + z2) * (kv ? 4096 : 6144) + cg * 1024;
  const u32 ub = opaque_s0(((kv ? 0x0c080400u : 0x100b0600u) >> (8 * rg + z2)) & 255);
  pr.va[0] = opaque((ub * 16 + 8 * cg + (lane >> 3)) * 1280 + (((lane & 7) ^ (lane >> 4) ^ (4 * cg)) * 16));
  pr.va[1] = opaque(pr.va[0] + 16 * 1280);
  asm volatile("" : "=v"(pr.va[2]));
  pr.sxa = p.x + ustart * (16 * 1280);
  pr.kind = idx - 3 * i3;
  const bool first = !kv && rg == 0, k0 = pr.kind == 0;
  pr.xmask = opaque_m(first && pr.kind != cg ? 0x100ull : ~0ull);
  pr.v0 = opaque(pr.va[0] + (first && k0 ? 22 * 16 * 1280 : 0));
  set_m0<0>(lx); glds_gom<0>(pr.v0, pr.sxa, pr.xmask); glds<2048, 2>(pr.va[1], pr.sxa, lx);
  STAMP(26);
  SB();
  *(volatile LDS3 u32*)(size_t)(L_GO + (lane & 7) * 4) = 0u;
  const int ti = 8 * (xcd >> 1) + (idx >> 2), tj = 4 * (xcd & 1) + (idx & 3);
  const __amdgpu_buffer_rsrc_t rws = __builtin_amdgcn_make_buffer_rsrc(p.ws, 0, WS_END, 0x00020000);
  const int v = w ^ SWAP;
#define KD(v) ((int)(0x45403b36302a241eull >> (8 * (v))) & 255)
  const u32 v16 = pr.l16;
  f32x4 R[28];
  bf16x8 BR[25];
#pragma unroll
  for (int i = 0; i < 28; i++) asm volatile("" : "=v"(R[i]));
#pragma unroll
  for (int i = 25 - NEX; i < 25; i++) asm volatile("" : "=v"(BR[i]));
  Late lt;
  asm volatile("" : "=s"(lt.w2t), "=s"(lt.pb), "=s"(lt.f1), "=s"(lt.eo), "=s"(lt.nonce));
  auto late = [&](auto k) {
    const u32 z = opaque_s0(0), st = strip + z;
    if constexpr (k.v == 0) lt.w2t = opaque_p(p.ws + (WS_W2T + (tj + z) * W2T_STRIDE));
    if constexpr (k.v == 1) lt.pb = opaque_p(lt.w2t + KD(v + z) * 5120);
    if constexpr (k.v == 2) { const u32 m = st < 27 ? 0 : 1; lt.eo = opaque_s0((m ? WS_PQ - 108 * 1024 : WS_HF) + ((ustart + ub + z) * (m ? 20 : 74) + (st << (1 + m)) + cg) * 1024); }
    if constexpr (k.v == 3) { const u32 fk = g * 8 + rt8 + z, ff = (WS_F1 - WS_F1KV) / FS + rt * 32 + st; lt.f1 = opaque_p(p.ws + (WS_F1KV + (kv ? fk : ff) * FS)); }
  };
  const u32 role = opaque_s0(((kv ? 0xaau : 0x14u) >> (2 * rg)) & 3);
  if (opaque_s0(role) == 0) ffn_loop<6>(p, rws, b, w, lane, ustart, rstride, pr, lt, late, R, BR STA);
  asm volatile("; role boundary");
  if (opaque_s0(role) == 1) ffn_loop<5>(p, rws, b, w, lane, ustart, rstride, pr, lt, late, R, BR STA);
  asm volatile("; role boundary");
  if (opaque_s0(role) == 2) kv_loop(p, b, w, lane, g, rt8, pr, lt, late, R, BR STA);
  const char *w2t = lt.w2t, *pb = lt.pb;
  asm volatile("s_nop 7" ::: "memory");
  STAMP(3);
#pragma unroll
  for (int i = 0; i < 25 - NEX; i++) asm volatile("" : "=v"(BR[i]));
  SB();
  if (opaque_s0(role) != 2) {
    ffn_epilogue(rws, w, v16, role, ub, pr.kind, lt.eo, strip, w2t, pb, R, BR);
#pragma unroll
    for (int i = 0; i < 25; i++) asm volatile("" : "=v"(R[i]));
  }
  asm volatile("; epi boundary");
  if (opaque_s0(role) == 2) kv_epilogue(rws, w, v16, g, rt8, w2t, pb, R, BR);
  asm volatile("; epi end");
  STAMP(4);
  const u32 nonce = lt.nonce;
  const u32 dsite = opaque_s0(((strip >= 27 ? 0xf0u : FD * 0x0fu) >> w) & 1);
  if (dsite) static_for<0, NK>([&](auto i) { ldx<i.v>(BR[i.v], v16, pb); });
#define KUSED(t, s) (!((t) < 2 && (s) == 2) && !(((t) == 3 || (t) == 4) && (s) == 0))
  u32 ka[6][3][2];
#define KA_UNDEF() do { _Pragma("unroll") for (int i = 0; i < 36; i++) if (KUSED(i / 6, (i >> 1) % 3)) asm volatile("" : "=v"(ka[i / 6][(i >> 1) % 3][i & 1])); } while (0)
  KA_UNDEF();
  if (opaque_s0(v) >= 4) {
    const int lz = opaque(v16), r = (lz >> 4) & 15, q8 = (lz >> 8) * 8 - 32 * (tj & 1);
#pragma unroll
    for (int t = 0; t < 6; t++) {
      const int n = 16 * t + r, hh = t == 5 ? (r & 3) : t == 0 ? 0 : t == 4 ? 3 : t - (r < 4 * t);
      const int lo = L_KVT - 8 + (t == 5 ? hh * 21 + 20 : n + hh) * 48, hi = lo + 48, x0 = opaque(lo + 8 + q8 - 40 * hh);
#pragma unroll
      for (int j = 0; j < 6; j++) if (KUSED(t, j >> 1)) asm volatile("v_med3_i32 %0, %1, %2, %3" : "=v"(ka[t][j >> 1][j & 1]) : "v"(x0 + 32 * j), "v"(lo), "v"(hi));
    }
    }
  sync_vm2<0, NK, 1>(dsite);
  STAMP(5);
  if (v == 3) st_flag(lt.f1, nonce);
  const int s0 = (80 * tj) >> 5, kd = KD(v);
  const u32 bo = WS_W2T + tj * W2T_STRIDE + kd * 5120;
#define LDB(i) __builtin_bit_cast(bf16x8, __builtin_amdgcn_raw_buffer_load_b128(rws, v16, bo + (i) * 1024, 16))
  const __amdgpu_buffer_rsrc_t rx = __builtin_amdgcn_make_buffer_rsrc((void*)p.x, 0, 2048 * 1280, 0x00020000);
  const u32 xso = ti * (64 * 1280) + tj * 160;
  const __amdgpu_buffer_rsrc_t ro = __builtin_amdgcn_make_buffer_rsrc(p.out, 0, 2048 * 1280, 0x00020000);
  u32x4 xr0;
  u32 ra0, go0;
  asm volatile("" : "=v"(xr0), "=v"(ra0), "=v"(go0));
#define GET(a, c) f32x4 pp[8]; _Pragma("unroll") for (int k = 0; k < 8; k++) pp[k] = ldsf4((k & 4 ? c : a) + (k & 2) * 15360 + (k & 1) * 16); SB()
#define OUT(xr, go) do { const f32x4 lo = (pp[0] + pp[2]) + (pp[4] + pp[6]), hi = (pp[1] + pp[3]) + (pp[5] + pp[7]); \
    st16<OAUX>(u32x4{pk2(lo[0] + blo(xr[0]), lo[1] + bhi(xr[0])), pk2(lo[2] + blo(xr[1]), lo[3] + bhi(xr[1])),  \
                     pk2(hi[0] + blo(xr[2]), hi[1] + bhi(xr[2])), pk2(hi[2] + blo(xr[3]), hi[3] + bhi(xr[3]))}, ro, go, xso); } while (0)
#define ITEM(id, ra, go, xr) do { const u32 r_ = ((id) * 6554) >> 16; ra = opaque((id) * 32 + r_ * 16); go = opaque((id) * 16 + r_ * 1120); \
    xr = __builtin_amdgcn_raw_buffer_load_b128(rx, go, xso, 0); } while (0)
#define LDS_WAIT(a, n_) lds_wait<1>(a, n_)
#define LDS_INC(a) asm volatile("s_waitcnt lgkmcnt(0)\n\ts_mov_b64 exec, 1\n\tds_add_u32 %0, %1\n\ts_mov_b64 exec, -1" ::"v"(a), "v"(1u) : "memory")
  if (opaque_s0(v) < 4) {
    if (QALL ? strip < 27 : !kv) static_for<NK, NK + 10>([&](auto i) { ldx<i.v>(BR[i.v], v16, pb); });
    if (!opaque_s0(dsite)) static_for<0, NK>([&](auto i) { ldx<i.v>(BR[i.v], v16, pb); });
    if (strip >= 27) {
      static_for<0, 5>([&](auto i) { set_m0<(120 + i.v) * 1024>(v * 5120); glds_lin(v16, w2t); });
      if (v < 2) static_for<0, 5>([&](auto i) { set_m0<(140 + i.v) * 1024>(v * 5120); glds_lin(v16, w2t); });
    }
    const u32 ao = WS_HF + 4 * ti * 74 * 1024, ao_d = ao + kd * 1024, ao_l = ao + 6 * v * 1024, ao_x = ao + (24 + v) * 1024, ao_a = ao + (54 + 5 * v) * 1024;
    const u32 bl = opaque(v * 30720 + v16), bx = opaque((120 + 5 * v) * 1024 + v16);
    bf16x8 A[19][4], B[20][5];
    f32x4 acc[4][5];
#pragma unroll
    for (int u = 0; u < 4; u++)
#pragma unroll
      for (int t = 0; t < 5; t++) { acc[u][t] = f32x4{0.f, 0.f, 0.f, 0.f}; pin(acc[u][t]); }
#define LA1(s, u) A[s][u] = __builtin_bit_cast(bf16x8, __builtin_amdgcn_raw_buffer_load_b128(rws, v16, \
      ((s) >= 14 ? ao_a + srank(s) * 1024 : (s) == 0 ? ao_x + 4096 : STYPE[s] != 'L' ? ao_d + srank(s) * 1024 : srank(s) < 6 ? ao_l + srank(s) * 1024 : ao_x) + (u) * 74 * 1024, 16))
    const bool has0 = v < 2;
    {
      const int ln = opaque(v16) >> 4, l7 = ln & 7, uu = 4 * ti + (ln & 8 ? 3 : 0), ul = uu & 63;
      const int s1 = l7 < 3 ? 3 * v + l7 : 12 + (v >> 1), s2 = l7 > 4 ? 3 * v + 10 + l7 : 14, r3 = (uu >> 6) * 3 + (ul >= 22) + (ul >= 43);
      poll16(p.ws, WS_F1 + (r3 * 32 + (l7 < 4 ? s1 : s2)) * FS, 0xffff, nonce);
    }
    asm volatile("; gemm poll done");
    STAMP(8);
    SB();
    constexpr Stream SQ = make_stream();
    u32 pa;
    asm volatile("" : "=v"(A[0][0]), "=v"(A[0][1]), "=v"(A[0][2]), "=v"(A[0][3]));
    if (has0) { LA1(0, 0); LA1(0, 1); LA1(0, 2); LA1(0, 3); }
#define ISSUE(i)                                                                                              \
    do { if constexpr ((i) < SQ.n) {                                                                          \
        constexpr int s_ = SQ.e[(i) < SQ.n ? (i) : 0] >> 4, k_ = SQ.e[(i) < SQ.n ? (i) : 0] & 15;             \
        if constexpr (s_ == 14 && k_ == 0) {                                                                  \
          const u32 tid = v * 64 + (opaque(v16) >> 4);                                                        \
          pa = opaque(v * 30720 + (tid & 15) * 336 + (tid & 48));                                             \
          ITEM(tid + 128, ra0, go0, xr0);                                                                     \
          STAMP(9); LDS_WAIT(L_GO, 5u); STAMP(10);                                                            \
        }                                                                                                     \
        if constexpr (k_ < 4) LA1(s_, k_); else B[s_][k_ - 4] = LDB(srank(s_) * 5 + k_ - 4);                  \
      } } while (0)
    static_for<0, WIN - 4>([&](auto i) { ISSUE(i.v); });
    SB();
    if (has0) {
#pragma unroll
      for (int t = 0; t < 5; t++) B[0][t] = lds128(bx + (20 + t) * 1024);
#pragma unroll
      for (int u = 0; u < 4; u++)
#pragma unroll
        for (int t = 0; t < 5; t++) mfma_ip(acc[u][t], B[0][t], A[0][u]);
    }
    SB();
    static_for<WIN - 4, WIN>([&](auto i) { ISSUE(i.v); });
    SB();
    static_for<1, 19>([&](auto sc) {
      static_for<0, 20>([&](auto mc) {
        constexpr int i = sc.v * 20 + mc.v, s = mstep(i), u = munit(i), t = i % 5;
        if constexpr (STYPE[s] == 'R') mfma_ip(acc[u][t], BR[srank(s) * 5 + t], A[s][u]);
        else mfma_ip(acc[u][t], B[s][t], A[s][u]);
        SB();
        static_for<SQ.upto[i - 1], SQ.upto[i]>([&](auto k) { ISSUE(k.v); });
        if constexpr (s < 15 && STYPE[s + 1] == 'L' && u == 2) B[s + 1][t] = lds128(srank(s + 1) < 6 ? bl + (srank(s + 1) * 5 + t) * 1024 : bx + t * 1024);
        if constexpr (s == 15 && u >= 1) B[15 + u][t] = lds128(bl + (5 + 5 * u + t) * 1024);
        if constexpr (s < 14 && STYPE[s] == 'L' && (srank(s) == 4 || srank(s) == (NP1 - 1) / 5) && mc.v == 5) *(volatile LDS3 u32*)(size_t)(L_FREE + v * 4) = srank(s) == 4 ? 2u : 1u;
        if constexpr (i >= 338 && (i - 338) % 15 < 10 && (i - 338) % 15 % 2 == 0) {
          constexpr int uw = (i - 338) / 15, tw = (i - 338) % 15 / 2;
          *(LDS3 f32x4*)(size_t)(pa + uw * (16 * 336) + tw * 64) = acc[uw][tw];
        }
        if constexpr (i == 347) asm volatile("s_mov_b64 exec, 1\n\tds_add_u32 %0, %1\n\ts_mov_b64 exec, -1" ::"v"(L_GO0), "v"(1u) : "memory");
        SB();
      });
    });
    asm volatile("" : "+v"(xr0));
#undef ISSUE
#undef LA1
    STAMP(11);
    SB(); asm volatile("s_nop 3" ::: "memory"); SB();
#pragma unroll
    for (int t = 0; t < 5; t++) { *(LDS3 f32x4*)(size_t)(pa + 3 * (16 * 336) + t * 64) = acc[3][t]; SB(); }
#pragma unroll
    for (int i = 0; i < 25; i++) asm volatile("" : "=v"(BR[i]));
    KA_UNDEF();
  }
  if (opaque_s0(v) >= 4) {
    const int ln = opaque(v16) >> 4, r = ln & 15, q = ln >> 4, hw = v - 4, th = hw * 64 + ln, ug = 4 * ti + hw;
    SETPRIO(3);
    __builtin_amdgcn_s_waitcnt(0x0F70);
    const u32 so2 = opaque((q & 1) ? 0x80000000u : (q >> 1) * 256 + r * 16);
    SB();
    const u32 ob = opaque_s0(WS_HF + (ug * 74 + 54 + 5 * (tj >> 1)) * 1024), po = opaque_s0(WS_KVP + tj * 8 * KVP_STRIDE + hw * 1024);
    const u32 qo = opaque_s0(WS_PQ + (ug * 20 + s0) * 1024);
    const char* f2 = opaque_p(p.ws + WS_F2 + (ti * 32 + hw * 8 + tj) * FS);
    bf16x8 sel[2];
    {
      const u32 one = (r >> 2) == q ? 0x3f80u << (16 * (r & 1)) : 0u, a = r & 2 ? 0u : one, c = r & 2 ? one : 0u;
      sel[0] = __builtin_bit_cast(bf16x8, u32x4{a, c, 0u, 0u}); sel[1] = __builtin_bit_cast(bf16x8, u32x4{0u, 0u, a, c});
      asm volatile("" : "+v"(sel[0]), "+v"(sel[1]));
    }
    u32 kw0, kw1;
    {
      const int tile = 2 * (hw & 1) + (ln >> 5), q2 = (ln >> 3) & 3, r2 = 2 * (ln & 7);
      const bool coded = tile == 2 || (tile == 3 && q2 >= 2);
      const int hb = tile < 2 ? tile : tile == 2 ? (r2 >> 2) & 1 : q2 & 1;
      const bool mine = !coded || ((r2 >> 2) & 1) == hb;
      const int e = !coded ? r2 : r2 >= 8 ? 16 + (r2 & 3) : 20;
      const u32 a = L_KVT + ((2 * (hw >> 1) + hb) * 21 + e) * 48 + (tile == 3 ? 4 : q2) * 8;
      kw0 = opaque(mine && (!coded || r2 >= 8 || !(r2 & 3)) ? a : L_JUNK);
      kw1 = opaque(mine && (!coded || r2 >= 8) ? a + 48 : L_JUNK);
      *(LDS3 u32x2*)(size_t)(th < 85 ? L_KVT - 8 + th * 48 : L_JUNK) = u32x2{0u, 0u};
    }
    STAMP(14);
    {
      const int ul = ug & 63, r3 = (ug >> 6) * 3 + (ul >= 22 - ((ln >> 1) & 1)) + (ul >= 43);
      poll16(p.ws, ln < 8 ? WS_F1KV + (tj * 8 + ln) * FS : WS_F1 + (r3 * 32 + 27 + ((s0 + 2 * (ln & 1)) >> 2)) * FS, 0xfff, nonce);
    }
    STAMP(6);
    u32x4 pv[8];
    bf16x8 pq[3];
#pragma unroll
    for (int k = 0; k < 8; k++) { pv[k] = __builtin_amdgcn_raw_buffer_load_b128(rws, v16, po + k * KVP_STRIDE, 16); SB(); }
#pragma unroll
    for (int s = 0; s < 3; s++) pq[s] = __builtin_bit_cast(bf16x8, __builtin_amdgcn_raw_buffer_load_b128(rws, v16, qo + s * 1024, 16));
    SB();
    {
      f32x4 a0 = {0.f, 0.f, 0.f, 0.f}, a1 = a0;
#pragma unroll
      for (int k = 0; k < 8; k++) { a0 = mfma(sel[0], __builtin_bit_cast(bf16x8, pv[k]), a0); a1 = mfma(sel[1], __builtin_bit_cast(bf16x8, pv[k]), a1); SB(); }
      *(LDS3 u32x2*)(size_t)kw0 = u32x2{pk2(a0[0], a0[1]), pk2(a0[2], a0[3])};
      *(LDS3 u32x2*)(size_t)kw1 = u32x2{pk2(a1[0], a1[1]), pk2(a1[2], a1[3])};
    }
    STAMP(7);
    asm volatile("s_mov_b64 exec, 1\n\tds_add_u32 %0, %1\n\ts_mov_b64 exec, -1" ::"v"(L_CNT), "v"(1u) : "memory");
    lds_wait<0>(L_CNT, 4u);
    STAMP(8);
    bf16x8 kf[6][3];
#pragma unroll
    for (int t = 5; t >= 0; t--)
#pragma unroll
      for (int s = 0; s < 3; s++)
        if (KUSED(t, s)) kf[t][s] = __builtin_shufflevector(*(const LDS3 s16x4*)(size_t)ka[t][s][0], *(const LDS3 s16x4*)(size_t)ka[t][s][1], 0, 1, 2, 3, 4, 5, 6, 7);
    SB();
    f32x4 nm[6];
#pragma unroll
    for (int t = 5; t >= 0; t--) {
      mfma_c<true>(nm[t], kf[t][1], pq[1]);
      if (KUSED(t, 0)) mfma_ip(nm[t], kf[t][0], pq[0]);
      if (KUSED(t, 2)) mfma_ip(nm[t], kf[t][2], pq[2]);
    }
    SB();
    float inv[4];
#pragma unroll
    for (int i = 0; i < 4; i++) inv[i] = __builtin_amdgcn_rcpf(__builtin_bit_cast(float, opaque(__builtin_bit_cast(u32, nm[5][i] + 1e-6f))));
    const float iv[5] = {inv[0], q < 1 ? inv[0] : inv[1], q < 2 ? inv[1] : inv[2], q < 3 ? inv[2] : inv[3], inv[3]};
    asm volatile("" ::"v"(iv[1]), "v"(iv[2]), "v"(iv[3]));
    SB();
    u32 od[5][2];
#pragma unroll
    for (int t = 4; t >= 0; t--) { od[t][0] = pk2(mul1(nm[t][0], iv[t]), mul1(nm[t][1], iv[t])); od[t][1] = pk2(mul1(nm[t][2], iv[t]), mul1(nm[t][3], iv[t])); }
#define PUT_O(a, c, e, off)                                                                                   \
    do { auto e0 = __builtin_amdgcn_permlane16_swap(od[e][0], od[e][0], false, false);                        \
         auto e1 = __builtin_amdgcn_permlane16_swap(od[e][1], od[e][1], false, false);                        \
         st16<16>(u32x4{od[c][0], od[c][1], od[c + 1][0], od[c + 1][1]}, rws, v16, ob + (off) * 3072 + 1024); \
         st16<16>(u32x4{od[a][0], od[a][1], od[a + 1][0], od[a + 1][1]}, rws, v16, ob + (off) * 3072);        \
         st16<16>(u32x4{e0[0], e1[0], e0[1], e1[1]}, rws, so2, ob + 2048 + (off) * 512); } while (0)
    if (tj & 1) PUT_O(1, 3, 0, 1); else PUT_O(0, 2, 4, 0);
    SETPRIO(PUTPRIO);
    const u32 ba = opaque(hw * 30720 + v16);
#define PUT_B(a, e) do { _Pragma("unroll") for (int i = a; i < e; i++) *(LDS3 bf16x8*)(size_t)(ba + i * 1024) = BR[i]; } while (0)
    if (NP1) { LDS_WAIT(L_FREE + hw * 4, 1u); PUT_B(0, NP1); }
    wait_vm<0>();
    st_flag(f2, nonce);
    STAMP(9);
    LDS_WAIT(L_FREE + hw * 4, 2u);
    PUT_B(NP1, 25);
    LDS_INC(L_GO);
    if (v == 4) {
      poll16(p.ws, WS_F2 + (ti * 32 + (ln & 31)) * FS, 0xffffffffull, nonce);
      LDS_INC(L_GO);
    }
    STAMP(10);
    ITEM(th + 384, ra0, go0, xr0);
    asm volatile("" : "+v"(xr0));
    if (hw < 2) {
      u32x4 xr1;
      u32 ra1, go1;
      ITEM(th, ra1, go1, xr1);
      const u32 rb1 = opaque(ra1 + 61440);
      float xf[8];
#pragma unroll
      for (int i = 0; i < 8; i++) { xf[i] = i & 1 ? bhi(xr1[i >> 1]) : blo(xr1[i >> 1]); asm volatile("" : "+v"(xf[i])); }
      SETPRIO(0);
      LDS_WAIT(L_GO0, 4u);
      GET(ra1, rb1);
      float y[8];
#pragma unroll
      for (int i = 0; i < 8; i++) {
        const int h = i >> 2, e = i & 3;
        y[i] = add1(add1(add1(pp[h][e], pp[2 + h][e]), add1(pp[4 + h][e], pp[6 + h][e])), xf[i]);
      }
      st16<OAUX>(u32x4{pk2(y[0], y[1]), pk2(y[2], y[3]), pk2(y[4], y[5]), pk2(y[6], y[7])}, ro, go1, xso);
    }
  }
#undef LDB
  const u32 rb0 = opaque(ra0 + 61440);
  sync_lds();
  STAMP(12);
  GET(ra0, rb0);
  OUT(xr0, go0);
  STAMP(13);
  STAMP(31);
  STAMP_FLUSH();
}

extern "C" void kernel_launch(void* const* d_in, const int* in_sizes, int n_in, void* d_out, int out_size, void* d_ws,
                              size_t ws_size, hipStream_t stream) {
  Params p;
  p.x = (const char*)d_in[0]; p.wqkv = (const char*)d_in[1]; p.wo = (const char*)d_in[2];
  p.wg = (const char*)d_in[4]; p.wu = (const char*)d_in[5]; p.wd = (const char*)d_in[6];
  p.out = (char*)d_out; p.ws = (char*)d_ws + WSOFF;
  fused_block<<<256, 512, 0, stream>>>(p);
}
```
